```python
import torch

def _softmax(x, axis=-1):
    return torch.softmax(x, dim=axis)
def _gelu(x):
    return torch.nn.functional.gelu(x, approximate="tanh")
def _silu(x):
    return torch.nn.functional.silu(x)
def _sigmoid(x):
    return torch.sigmoid(x)
def _layernorm(x, axis=-1, eps=1e-5):
    m = x.mean(dim=axis, keepdim=True)
    v = x.var(dim=axis, keepdim=True, unbiased=False)
    return (x - m) / torch.sqrt(v + eps)
def _scatter_add(tgt, idx, src, axis=0):
    return tgt.clone().index_add_(axis, idx, src)
def _segment_sum(src, seg, n):
    out = torch.zeros((n,) + src.shape[1:], dtype=src.dtype)
    return out.index_add_(0, seg, src)
def _pad(x, pw):
    flat = tuple(v for pair in reversed(pw) for v in pair)
    return torch.nn.functional.pad(x, flat)

kernel_name = 'parallel_sum__mha_block__dense_activation_gelu__prompt'

def setup_inputs():

    _w = (lambda a: (lambda t: t.to(torch.bfloat16) if t.is_floating_point() else t)(torch.as_tensor(a)))
    def _A_setup():
        B, S, D, H = 1, 2048, 448, 16
        return {
            "x": _w((lambda *s: torch.randn(*s, dtype=torch.bfloat16) * 0.05)(B, S, D)),
            "wqkv": _w((lambda *s: torch.randn(*s, dtype=torch.bfloat16) * 0.05)(D, 3 * D)),
            "wo": _w((lambda *s: torch.randn(*s, dtype=torch.bfloat16) * 0.05)(D, D)),
        }
    _A_in = _A_setup()

    def _B_setup():
        B, S, D, F = 1, 2048, 448, 1792
        return {
            "x": _w((lambda *s: torch.randn(*s, dtype=torch.bfloat16) * 0.05)(B, S, D)),
            "w1": _w((lambda *s: torch.randn(*s, dtype=torch.bfloat16) * 0.8)(D, F) * (D ** -0.5)),
            "b1": _w((lambda *s: torch.randn(*s, dtype=torch.bfloat16) * 12.8)(F) * 0.01),
            "w2": _w((lambda *s: torch.randn(*s, dtype=torch.bfloat16) * 0.4)(F, D) * (F ** -0.5)),
            "b2": _w((lambda *s: torch.randn(*s, dtype=torch.bfloat16) * 6.4)(D) * 0.01),
        }
    _B_in = _B_setup()

    return {
        "A_x": _A_in["x"],
        "A_wqkv": _A_in["wqkv"],
        "A_wo": _A_in["wo"],
        "B_x": _B_in["x"],
        "B_w1": _B_in["w1"],
        "B_b1": _B_in["b1"],
        "B_w2": _B_in["w2"],
        "B_b2": _B_in["b2"],
    }

def reference(A_x, A_wqkv, A_wo, B_x, B_w1, B_b1, B_w2, B_b2):
    def _A_ref(x, wqkv, wo):
        B, S, D = x.shape
        H = 16
        Dh = D // H
        qkv = (lambda x, *d: x.permute(*d))((x @ wqkv).reshape(B, S, 3, H, Dh), 2, 0, 3, 1, 4)
        q, k, v = qkv[0], qkv[1], qkv[2]
        scores = torch.einsum("bhqd,bhkd->bhqk", q, k) * (Dh ** -0.5)
        p = _softmax(scores, axis=-1)
        o = torch.einsum("bhqk,bhkd->bhqd", p, v)
        return (lambda x, *d: x.permute(*d))(o, 0, 2, 1, 3).reshape(B, S, D) @ wo

    def _B_ref(x, w1, b1, w2, b2):
        h = torch.einsum("bsd,df->bsf", x, w1) + b1
        h = _gelu(h)
        return torch.einsum("bsf,fd->bsd", h, w2) + b2

    x = A_x
    xn = _layernorm(x, axis=-1)
    return x + _A_ref(xn, A_wqkv, A_wo) + _B_ref(xn, B_w1, B_b1, B_w2, B_b2)
```

```cpp
#include <hip/hip_runtime.h>
#include <hip/hip_bf16.h>
#include <stdint.h>
#include <type_traits>

#define DI __device__ __forceinline__
#define LAS __attribute__((address_space(3)))
typedef float f32x16 __attribute__((ext_vector_type(16)));
typedef float f32x4 __attribute__((ext_vector_type(4)));
typedef float f32x2 __attribute__((ext_vector_type(2)));
typedef __bf16 bf16x8 __attribute__((ext_vector_type(8)));
typedef __bf16 bf16x2 __attribute__((ext_vector_type(2)));
typedef unsigned u32x4 __attribute__((ext_vector_type(4)));
typedef unsigned u32x2 __attribute__((ext_vector_type(2)));
typedef short s16x4 __attribute__((ext_vector_type(4)));
typedef __amdgpu_buffer_rsrc_t rsrc_t;

constexpr int SEQ = 2048, DM = 448, NH = 16, DH = 28, FF = 1792;
constexpr unsigned WS_KN2 = 512u << 10;
constexpr unsigned WS_Q = 1u << 20, WS_K = 3u << 20, WS_V = 5u << 20, WS_O = 7u << 20, WS_H = 9u << 20;
constexpr unsigned WS_BYTES = 17u << 20;
constexpr float C2 = 0.27264234f;
constexpr float LN_EPS = 1e-5f;
constexpr int AUX_SC1 = 16;
constexpr unsigned LDS_BYTES = 160 * 1024;
constexpr unsigned TSLOT = 28 * 1024;

DI rsrc_t mk_rsrc(const void* p, unsigned bytes) { return __builtin_amdgcn_make_buffer_rsrc((void*)p, (short)0, (int)bytes, 0x00020000); }
DI unsigned rfl(unsigned x) { return __builtin_amdgcn_readfirstlane(x); }
DI void dma16(rsrc_t rs, unsigned voff, unsigned soff, unsigned ldsaddr) {
    asm volatile("s_nop 4\n\ts_mov_b32 m0, %0\n\ts_nop 0\n\tbuffer_load_dwordx4 %1, %2, %3 offen lds" ::"s"(ldsaddr), "v"(voff), "s"(rs), "s"(soff) : "memory");
}
#define WAIT_VM0() asm volatile("s_waitcnt vmcnt(0)" ::: "memory")
#define BAR() asm volatile("s_waitcnt lgkmcnt(0)\n\ts_barrier" ::: "memory")
#define VM0_BAR() asm volatile("s_waitcnt vmcnt(0) lgkmcnt(0)\n\ts_barrier" ::: "memory")

DI f32x16 mfma16(u32x4 a, u32x4 b, f32x16 c) { return __builtin_amdgcn_mfma_f32_32x32x16_bf16(__builtin_bit_cast(bf16x8, a), __builtin_bit_cast(bf16x8, b), c, 0, 0, 0); }
DI f32x16 mfma_f32(float a, float b, f32x16 c) { return __builtin_amdgcn_mfma_f32_32x32x2f32(a, b, c, 0, 0, 0); }
DI unsigned cvtpk(float lo, float hi) { f32x2 v = {lo, hi}; return __builtin_bit_cast(unsigned, __builtin_convertvector(v, bf16x2)); }
DI float dot2(unsigned a, unsigned b, float c) { return __builtin_amdgcn_fdot2_f32_bf16(__builtin_bit_cast(bf16x2, a), __builtin_bit_cast(bf16x2, b), c, false); }
DI float swap_add(float x) { auto r = __builtin_amdgcn_permlane32_swap(__float_as_uint(x), __float_as_uint(x), false, false); return __uint_as_float(r[0]) + __uint_as_float(r[1]); }
DI float swap_max(float x) { auto r = __builtin_amdgcn_permlane32_swap(__float_as_uint(x), __float_as_uint(x), false, false); return fmaxf(__uint_as_float(r[0]), __uint_as_float(r[1])); }
DI u32x4 tr_frag(LAS char* p) {
    s16x4 lo = __builtin_amdgcn_ds_read_tr16_b64_v4i16((LAS s16x4*)p), hi = __builtin_amdgcn_ds_read_tr16_b64_v4i16((LAS s16x4*)(p + 256));
    u32x2 a = __builtin_bit_cast(u32x2, lo), b = __builtin_bit_cast(u32x2, hi);
    return u32x4{a[0], a[1], b[0], b[1]};
}
DI u32x4 tr_frag_perm(LAS char* p) {
    s16x4 lo = __builtin_amdgcn_ds_read_tr16_b64_v4i16((LAS s16x4*)p), hi = __builtin_amdgcn_ds_read_tr16_b64_v4i16((LAS s16x4*)(p + 512));
    u32x2 a = __builtin_bit_cast(u32x2, lo), b = __builtin_bit_cast(u32x2, hi);
    return u32x4{a[0], a[1], b[0], b[1]};
}
DI u32x4 pack8(const f32x16& v, int s) { return u32x4{cvtpk(v[8 * s], v[8 * s + 1]), cvtpk(v[8 * s + 2], v[8 * s + 3]), cvtpk(v[8 * s + 4], v[8 * s + 5]), cvtpk(v[8 * s + 6], v[8 * s + 7])}; }
DI void st16(rsrc_t rs, unsigned off, u32x4 v) { __builtin_amdgcn_raw_buffer_store_b128(v, rs, off, 0, AUX_SC1); }
DI u32x4 ld16(rsrc_t rs, unsigned voff, unsigned soff) { return __builtin_amdgcn_raw_buffer_load_b128(rs, voff, soff, AUX_SC1); }
DI float bf2f(unsigned short b) { return __uint_as_float((unsigned)b << 16); }

struct Ptrs { const void* x; const void* wqkv; const void* wo; const void* w1; const void* b1; const void* w2; const void* b2; void* out; char* ws; };

template <bool SWAP> DI void p1_kloop(LAS char* wp, const u32x4 (&xf)[28], f32x16& acc, float& cs) {
#pragma unroll
    for (int s = 0; s < 28; ++s) {
        const u32x4 wf = tr_frag(wp + s * 1024);
        acc = SWAP ? mfma16(xf[s], wf, acc) : mfma16(wf, xf[s], acc);
        cs = dot2(wf[0], 0x3F803F80u, cs); cs = dot2(wf[1], 0x3F803F80u, cs); cs = dot2(wf[2], 0x3F803F80u, cs); cs = dot2(wf[3], 0x3F803F80u, cs);
    }
}

DI void phase1(const Ptrs& P, LAS char* lds, int b) {
    const unsigned tid = threadIdx.x, lane = tid & 63, l31 = lane & 31, h = lane >> 5;
    const unsigned w = rfl(tid >> 6), tt = w & 3, half = w >> 2;
    const unsigned g = b & 7, r = b >> 3, tb = 4 * (g >> 1) + (r & 3), cg = 8 * (g & 1) + (r >> 2);
    const unsigned ntile = 7 - (cg & 1), fstart = 7 * (cg >> 1) + (cg & 1) * 4;
    const unsigned lds0 = (unsigned)(size_t)lds;
    const rsrc_t rx = mk_rsrc(P.x, SEQ * DM * 2), rqkv = mk_rsrc(P.wqkv, DM * 3 * DM * 2), rw1 = mk_rsrc(P.w1, DM * FF * 2), rws = mk_rsrc(P.ws, WS_BYTES);
    LAS float* stat = (LAS float*)(lds + 5 * TSLOT);
    auto dma_tile = [&](unsigned c, unsigned slot) {
        const bool qkv = c < 3;
        const unsigned ld2 = qkv ? 3 * DM * 2 : FF * 2, col0 = qkv ? (3 * cg + c) * 56 : (fstart + c - 3) * 64;
        const unsigned voff = (lane >> 2) * ld2 + (lane & 3) * 16 + col0;
        for (unsigned s = w; s < 28; s += 8) {
            if (qkv) dma16(rqkv, voff, s * 16 * ld2, lds0 + slot * TSLOT + s * 1024);
            else dma16(rw1, voff, s * 16 * ld2, lds0 + slot * TSLOT + s * 1024);
        }
    };
    for (unsigned i = 0; i < 14; ++i) {
        const unsigned blk = w + 8 * i, p = blk * 64 + lane, row = p / 56, c = p - row * 56;
        dma16(rx, (tb * 128 + row) * 896 + ((c ^ ((row >> 1) & 7)) << 4), 0, lds0 + TSLOT + blk * 1024);
    }
    dma_tile(0, 0);
    VM0_BAR();
    u32x4 xf[28];
    {
        LAS char* xb = lds + TSLOT + (tt * 32 + l31) * 896;
        const unsigned gsw = (l31 >> 1) & 7;
#pragma unroll
        for (int u = 0; u < 4; ++u) {
            LAS char* xu = xb + (((2 * u + h) ^ gsw) << 4);
#pragma unroll
            for (int m = 0; m < 7; ++m) xf[4 * m + u] = *(LAS u32x4*)(xu + 128 * m);
        }
    }
    {
        float s1 = 0.f, s2 = 0.f;
        if (half == 0) {
#pragma unroll
            for (int s = 0; s < 14; ++s)
#pragma unroll
                for (int e = 0; e < 4; ++e) { s1 = dot2(xf[s][e], 0x3F803F80u, s1); s2 = dot2(xf[s][e], xf[s][e], s2); }
        } else {
#pragma unroll
            for (int s = 14; s < 28; ++s)
#pragma unroll
                for (int e = 0; e < 4; ++e) { s1 = dot2(xf[s][e], 0x3F803F80u, s1); s2 = dot2(xf[s][e], xf[s][e], s2); }
        }
        *(LAS f32x2*)(stat + (tt * 32 + l31) * 8 + (half * 2 + h) * 2) = f32x2{s1, s2};
    }
    BAR();
    dma_tile(1, 1); dma_tile(2, 2); dma_tile(3, 3); dma_tile(4, 4);
    float mu, sig, rstd;
    {
        const f32x4 a = *(LAS f32x4*)(stat + (tt * 32 + l31) * 8), c = *(LAS f32x4*)(stat + (tt * 32 + l31) * 8 + 4);
        mu = (a[0] + a[2] + c[0] + c[2]) * (1.f / DM);
        const float var = fmaxf((a[1] + a[3] + c[1] + c[3]) * (1.f / DM) - mu * mu, 0.f) + LN_EPS;
        rstd = __builtin_amdgcn_rsqf(var); sig = var * rstd;
    }
    const unsigned troff = (8 * (lane >> 5) + ((lane & 15) >> 2)) * 64 + ((lane >> 4) & 1) * 32 + (lane & 3) * 8;
    const unsigned tile = tb * 4 + tt;
    for (unsigned step = 0; step < 4; ++step) {
        const unsigned c = step == 0 ? (half ? 99u : 0u) : 2 * step - 1 + half;
        if (c < ntile) {
            const unsigned slot = c < 5 ? c : c - 5;
            LAS char* wp = lds + slot * TSLOT + troff;
            f32x16 acc = {}; float cs = 0.f;
            const unsigned t = 3 * cg + c, type = c < 3 ? (t >> 4) : 3, head = t & 15;
            if (type == 2) p1_kloop<true>(wp, xf, acc, cs); else p1_kloop<false>(wp, xf, acc, cs);
            cs = swap_add(cs);
            if (type == 2) {
                acc = mfma_f32(h ? 0.f : -mu, h ? 0.f : cs, acc);
                f32x16 R = {}; R = mfma_f32(h ? 0.f : rstd, h ? 0.f : 1.f, R);
                const float keep = l31 < 28 ? 1.f : 0.f, one = l31 == 28 ? 1.f : 0.f;
#pragma unroll
                for (int i = 0; i < 16; ++i) acc[i] = l31 < 28 ? acc[i] * R[i] : one;
                (void)keep;
                const unsigned off = WS_V + (head * 64 + tile) * 2048 + lane * 16;
                st16(rws, off, pack8(acc, 0)); st16(rws, off + 1024, pack8(acc, 1));
            } else if (type == 3) {
                const unsigned ft = fstart + c - 3;
                const float bias = bf2f(((const unsigned short*)P.b1)[ft * 32 + l31]);
                acc = mfma_f32(h ? bias : cs, h ? sig : -mu, acc);
#pragma unroll
                for (int i = 0; i < 16; ++i) {
                    const float xv = acc[i] * rstd, t2 = xv * xv, pl = __builtin_fmaf(t2, 0.1029432397f, 2.302208198f), z = xv * pl;
                    acc[i] = xv * __builtin_amdgcn_rcpf(1.f + __builtin_amdgcn_exp2f(-z));
                }
                const unsigned off = WS_H + (tile * 112 + 2 * ft) * 1024 + lane * 16;
                st16(rws, off, pack8(acc, 0)); st16(rws, off + 1024, pack8(acc, 1));
            } else {
                acc = mfma_f32(h ? 0.f : cs, h ? 0.f : -mu, acc);
                const float sc = type == 0 ? rstd * C2 : rstd;
#pragma unroll
                for (int i = 0; i < 16; ++i) acc[i] *= sc;
                if (h) { acc[12] = type == 1 ? 1.f : 0.f; acc[13] = 0.f; acc[14] = 0.f; acc[15] = 0.f; }
                if (type == 1) {
                    float n2 = 0.f;
#pragma unroll
                    for (int i = 0; i < 12; ++i) n2 = __builtin_fmaf(acc[i], acc[i], n2);
                    if (!h) {
#pragma unroll
                        for (int i = 12; i < 16; ++i) n2 = __builtin_fmaf(acc[i], acc[i], n2);
                    }
                    n2 = swap_add(n2);
#pragma unroll
                    for (int o = 16; o >= 1; o >>= 1) n2 = fmaxf(n2, __shfl_xor(n2, o));
                    if (lane == 0) __hip_atomic_store((float*)(P.ws + WS_KN2) + head * 64 + tile, n2, __ATOMIC_RELAXED, __HIP_MEMORY_SCOPE_AGENT);
                }
                const unsigned off = (type == 0 ? WS_Q : WS_K) + (head * 64 + tile) * 2048 + lane * 16;
                st16(rws, off, pack8(acc, 0)); st16(rws, off + 1024, pack8(acc, 1));
            }
        }
        VM0_BAR();
        if (step == 0) dma_tile(5, 0);
        if (step == 1 && ntile == 7) dma_tile(6, 1);
    }
}

DI void phase2(const Ptrs& P, LAS char* lds, int b) {
    const unsigned tid = threadIdx.x, lane = tid & 63, l31 = lane & 31, h = lane >> 5;
    const unsigned w = rfl(tid >> 6), qt = w & 3, kh = w >> 2;
    const unsigned g = b & 7, r = b >> 3, head = 2 * g + (r >> 4), qb = r & 15, tile = qb * 4 + qt;
    const rsrc_t rws = mk_rsrc(P.ws, WS_BYTES);
    const unsigned ksrc = WS_K + (head * 64 + kh * 32 + qt) * 2048, vsrc = WS_V + (head * 64 + kh * 32 + qt) * 2048;
    u32x4 st[4];
    auto stage_load = [&](unsigned c) {
        st[0] = ld16(rws, lane * 16, ksrc + c * 4 * 2048); st[1] = ld16(rws, lane * 16 + 1024, ksrc + c * 4 * 2048);
        st[2] = ld16(rws, lane * 16, vsrc + c * 4 * 2048); st[3] = ld16(rws, lane * 16 + 1024, vsrc + c * 4 * 2048);
    };
    auto stage_write = [&](unsigned buf) {
        LAS char* d = lds + buf * 32768 + w * 4096 + lane * 16;
#pragma unroll
        for (int i = 0; i < 4; ++i) *(LAS u32x4*)(d + i * 1024) = st[i];
    };
    stage_load(0);
    u32x4 qf0 = ld16(rws, lane * 16, WS_Q + (head * 64 + tile) * 2048), qf1 = ld16(rws, lane * 16 + 1024, WS_Q + (head * 64 + tile) * 2048);
    float kn2 = __hip_atomic_load((float*)(P.ws + WS_KN2) + head * 64 + lane, __ATOMIC_RELAXED, __HIP_MEMORY_SCOPE_AGENT);
#pragma unroll
    for (int o = 32; o >= 1; o >>= 1) kn2 = fmaxf(kn2, __shfl_xor(kn2, o));
    float qn2 = 0.f;
#pragma unroll
    for (int e = 0; e < 4; ++e) { qn2 = dot2(qf0[e], qf0[e], qn2); qn2 = dot2(qf1[e], qf1[e], qn2); }
    qn2 = swap_add(qn2);
    const float bound = __builtin_amdgcn_sqrtf(qn2 * kn2) * 1.01f + 0.01f;
    const bool safe = __all(bound <= 40.f);
    float mrun;
    if (safe) {
        const unsigned nb = cvtpk(-bound, 0.f) & 0xffffu;
        if (h) qf1[2] = (qf1[2] & 0xffff0000u) | nb;
        mrun = __uint_as_float((nb & 0x7fffu) << 16);
    } else mrun = -INFINITY;
    stage_write(0);
    stage_load(1);
    BAR();
    f32x16 o = {};
    const f32x16 zero = {};
    for (unsigned c = 0; c < 8; ++c) {
        if (c + 1 < 8) stage_write((c + 1) & 1);
        if (c + 2 < 8) stage_load(c + 2);
        LAS char* kv = lds + (c & 1) * 32768 + kh * 16384 + lane * 16;
        if (safe) {
#pragma unroll
            for (int t = 0; t < 4; ++t) {
                const u32x4 k0 = *(LAS u32x4*)(kv + t * 4096), k1 = *(LAS u32x4*)(kv + t * 4096 + 1024), v0 = *(LAS u32x4*)(kv + t * 4096 + 2048), v1 = *(LAS u32x4*)(kv + t * 4096 + 3072);
                f32x16 s = mfma16(k0, qf0, zero); s = mfma16(k1, qf1, s);
#pragma unroll
                for (int i = 0; i < 16; ++i) s[i] = __builtin_amdgcn_exp2f(s[i]);
                o = mfma16(v0, pack8(s, 0), o); o = mfma16(v1, pack8(s, 1), o);
            }
        } else {
            for (int t = 0; t < 4; ++t) {
                const u32x4 k0 = *(LAS u32x4*)(kv + t * 4096), k1 = *(LAS u32x4*)(kv + t * 4096 + 1024), v0 = *(LAS u32x4*)(kv + t * 4096 + 2048), v1 = *(LAS u32x4*)(kv + t * 4096 + 3072);
                f32x16 s = mfma16(k0, qf0, zero); s = mfma16(k1, qf1, s);
                float m = s[0];
#pragma unroll
                for (int i = 1; i < 16; ++i) m = fmaxf(m, s[i]);
                m = fmaxf(swap_max(m), mrun);
                const float f = __builtin_amdgcn_exp2f(mrun - m);
                mrun = m;
#pragma unroll
                for (int i = 0; i < 16; ++i) { s[i] = __builtin_amdgcn_exp2f(s[i] - m); o[i] *= f; }
                o = mfma16(v0, pack8(s, 0), o); o = mfma16(v1, pack8(s, 1), o);
            }
        }
        BAR();
    }
    LAS float* ex = (LAS float*)(lds + qt * 8192);
    if (kh == 1) {
#pragma unroll
        for (int i = 0; i < 16; ++i) ex[i * 64 + lane] = o[i];
        ex[16 * 64 + lane] = mrun;
    }
    BAR();
    if (kh == 0) {
        const float m2 = ex[16 * 64 + lane], m = fmaxf(mrun, m2), f1 = __builtin_amdgcn_exp2f(mrun - m), f2 = __builtin_amdgcn_exp2f(m2 - m);
#pragma unroll
        for (int i = 0; i < 16; ++i) o[i] = o[i] * f1 + ex[i * 64 + lane] * f2;
        auto sw = __builtin_amdgcn_permlane32_swap(__float_as_uint(o[12]), __float_as_uint(o[12]), false, false);
        const float l = __uint_as_float(sw[1]);
        const float il = __builtin_amdgcn_rcpf(l);
#pragma unroll
        for (int i = 0; i < 16; ++i) o[i] *= il;
        if (h) { o[12] = 0.f; o[13] = 0.f; o[14] = 0.f; o[15] = 0.f; }
        const unsigned off = WS_O + (tile * 16 + head) * 2048 + lane * 16;
        st16(rws, off, pack8(o, 0)); st16(rws, off + 1024, pack8(o, 1));
    }
}

DI void phase3(const Ptrs& P, LAS char* lds, int b) {
    const unsigned tid = threadIdx.x, lane = tid & 63, l31 = lane & 31, h = lane >> 5;
    const unsigned w = rfl(tid >> 6), tt = w & 1, q = w >> 1;
    const unsigned g = b & 7, r = b >> 3;
    if (r >= 28) return;
    const unsigned tb = 4 * g + (r & 3), cg = r >> 2, tile = tb * 2 + tt;
    const unsigned lds0 = (unsigned)(size_t)lds;
    const rsrc_t rw2 = mk_rsrc(P.w2, FF * DM * 2), rwo = mk_rsrc(P.wo, DM * DM * 2), rws = mk_rsrc(P.ws, WS_BYTES);
    auto dma_sb = [&](unsigned sb) {
        for (unsigned n = w; n < 72; n += 8) {
            const unsigned qq = n / 18, rem = n - qq * 18, i = rem >> 1, ct = rem & 1, j = 9 * sb + i;
            const unsigned dst = lds0 + (sb & 1) * 73728 + n * 1024, colb = (cg * 64 + ct * 32) * 2 + (lane & 3) * 16;
            if (j < 28) dma16(rw2, (lane >> 2) * (DM * 2) + colb, (28 * qq + j) * 16 * (DM * 2), dst);
            else {
                const unsigned ks = 8 * qq + (j - 28), hd = ks >> 1, d0 = (ks & 1) * 16;
                unsigned row = hd * 28 + d0 + (lane >> 2); row = row < DM ? row : DM - 1;
                dma16(rwo, row * (DM * 2) + colb, 0, dst);
            }
        }
    };
    dma_sb(0); dma_sb(1);
    const unsigned troff = (4 * (lane >> 5) + ((lane & 15) >> 2)) * 64 + ((lane >> 4) & 1) * 32 + (lane & 3) * 8;
    f32x16 acc0 = {}, acc1 = {};
    for (unsigned sb = 0; sb < 4; ++sb) {
        u32x4 af[9];
#pragma unroll
        for (int i = 0; i < 9; ++i) {
            const unsigned j = 9 * sb + i;
            const unsigned so = j < 28 ? WS_H + (tile * 112 + 28 * q + j) * 1024 : WS_O + (tile * 32 + 8 * q + (j - 28)) * 1024;
            af[i] = ld16(rws, lane * 16, so);
        }
        VM0_BAR();
        LAS char* wp = lds + (sb & 1) * 73728 + q * 18 * 1024 + troff;
#pragma unroll
        for (int i = 0; i < 9; ++i) {
            const u32x4 w0 = tr_frag_perm(wp + i * 2048), w1 = tr_frag_perm(wp + i * 2048 + 1024);
            acc0 = mfma16(w0, af[i], acc0); acc1 = mfma16(w1, af[i], acc1);
        }
        BAR();
        if (sb + 2 < 4) dma_sb(sb + 2);
    }
    LAS float* ex = (LAS float*)lds;
    float v[8];
    auto scatter = [&](auto QC) {
        constexpr int Q = decltype(QC)::value;
#pragma unroll
        for (int d = 0; d < 4; ++d) {
            if (d != Q) {
#pragma unroll
                for (int i = 0; i < 8; ++i) ex[(((tt * 4 + d) * 4 + Q) * 8 + i) * 64 + lane] = (d >> 1) ? acc1[8 * (d & 1) + i] : acc0[8 * (d & 1) + i];
            } else {
#pragma unroll
                for (int i = 0; i < 8; ++i) v[i] = (d >> 1) ? acc1[8 * (d & 1) + i] : acc0[8 * (d & 1) + i];
            }
        }
    };
    if (q == 0) scatter(std::integral_constant<int, 0>{}); else if (q == 1) scatter(std::integral_constant<int, 1>{});
    else if (q == 2) scatter(std::integral_constant<int, 2>{}); else scatter(std::integral_constant<int, 3>{});
    BAR();
#pragma unroll
    for (int s = 1; s < 4; ++s) {
        const unsigned src = (q + s) & 3;
#pragma unroll
        for (int i = 0; i < 8; ++i) v[i] += ex[(((tt * 4 + q) * 4 + src) * 8 + i) * 64 + lane];
    }
    const unsigned tok = tile * 32 + l31, n0 = cg * 64 + q * 16;
#pragma unroll
    for (int gq = 0; gq < 2; ++gq) {
        const unsigned n = n0 + 8 * gq + 4 * h;
        const u32x2 xr = *(const u32x2*)((const char*)P.x + (tok * DM + n) * 2), br = *(const u32x2*)((const char*)P.b2 + n * 2);
        const float y0 = v[4 * gq] + __uint_as_float(xr[0] << 16) + __uint_as_float(br[0] << 16), y1 = v[4 * gq + 1] + __uint_as_float(xr[0] & 0xffff0000u) + __uint_as_float(br[0] & 0xffff0000u);
        const float y2 = v[4 * gq + 2] + __uint_as_float(xr[1] << 16) + __uint_as_float(br[1] << 16), y3 = v[4 * gq + 3] + __uint_as_float(xr[1] & 0xffff0000u) + __uint_as_float(br[1] & 0xffff0000u);
        *(u32x2*)((char*)P.out + (tok * DM + n) * 2) = u32x2{cvtpk(y0, y1), cvtpk(y2, y3)};
    }
}

template <int PH> __global__ __launch_bounds__(512, 2) void fused_kernel(const void* x, const void* wqkv, const void* wo, const void* w1, const void* b1, const void* w2, const void* b2, void* out, char* ws) {
    extern __shared__ __attribute__((aligned(16))) char lds_raw[];
    LAS char* lds = (LAS char*)lds_raw;
    const Ptrs P{x, wqkv, wo, w1, b1, w2, b2, out, ws};
    if (PH == 1) phase1(P, lds, blockIdx.x);
    if (PH == 2) phase2(P, lds, blockIdx.x);
    if (PH == 3) phase3(P, lds, blockIdx.x);
}

extern "C" void kernel_launch(void* const* d_in, const int* in_sizes, int n_in, void* d_out, int out_size, void* d_ws, size_t ws_size, hipStream_t stream) {
    (void)in_sizes; (void)n_in; (void)out_size; (void)ws_size;
    (void)hipFuncSetAttribute((const void*)fused_kernel<1>, hipFuncAttributeMaxDynamicSharedMemorySize, LDS_BYTES);
    (void)hipFuncSetAttribute((const void*)fused_kernel<2>, hipFuncAttributeMaxDynamicSharedMemorySize, LDS_BYTES);
    (void)hipFuncSetAttribute((const void*)fused_kernel<3>, hipFuncAttributeMaxDynamicSharedMemorySize, LDS_BYTES);
    fused_kernel<1><<<256, 512, LDS_BYTES, stream>>>(d_in[0], d_in[1], d_in[2], d_in[4], d_in[5], d_in[6], d_in[7], d_out, (char*)d_ws);
    fused_kernel<2><<<256, 512, LDS_BYTES, stream>>>(d_in[0], d_in[1], d_in[2], d_in[4], d_in[5], d_in[6], d_in[7], d_out, (char*)d_ws);
    fused_kernel<3><<<256, 512, LDS_BYTES, stream>>>(d_in[0], d_in[1], d_in[2], d_in[4], d_in[5], d_in[6], d_in[7], d_out, (char*)d_ws);
}
```

```cpp
#include <hip/hip_runtime.h>
#include <hip/hip_bf16.h>
#include <stdint.h>
#include <type_traits>
#include <utility>

#define DI __device__ __forceinline__
#define LAS __attribute__((address_space(3)))
#define GAS __attribute__((address_space(1)))
typedef float f32x16 __attribute__((ext_vector_type(16)));
typedef float f32x4 __attribute__((ext_vector_type(4)));
typedef float f32x2 __attribute__((ext_vector_type(2)));
typedef __bf16 bf16x8 __attribute__((ext_vector_type(8)));
typedef __bf16 bf16x2 __attribute__((ext_vector_type(2)));
typedef unsigned u32x4 __attribute__((ext_vector_type(4)));
typedef unsigned u32x2 __attribute__((ext_vector_type(2)));
typedef short s16x4 __attribute__((ext_vector_type(4)));
typedef unsigned long long u64;
typedef __amdgpu_buffer_rsrc_t rsrc_t;

constexpr int SEQ = 2048, DM = 448, NH = 16, DH = 28, FF = 1792;
constexpr unsigned WS_EPOCH = 0, WS_FQKV = 4096, WS_FH = WS_FQKV + 256 * 64, WS_FO = WS_FH + 256 * 64;
constexpr unsigned WS_KN2 = 512u << 10;
constexpr unsigned WS_Q = 1u << 20, WS_K = 3u << 20, WS_V = 5u << 20, WS_O = 7u << 20, WS_H = 9u << 20;
constexpr unsigned WS_BYTES = 17u << 20;
constexpr float C2 = 0.27264234f;
constexpr float LN_EPS = 1e-5f;
constexpr int AUX_SC1 = 16;
constexpr unsigned LDS_BYTES = 160 * 1024;
constexpr unsigned SPIN_MAX = 200000;
constexpr unsigned OOB = 0x7ffffff0u;
#define SAFE_BOUND 40.f
#define XL 1
#define XB 1
#define XA 1

DI rsrc_t mk_rsrc(const void* p, unsigned bytes) { return __builtin_amdgcn_make_buffer_rsrc((void*)p, (short)0, (int)bytes, 0x00020000); }
DI unsigned rfl(unsigned x) { return __builtin_amdgcn_readfirstlane(x); }
DI unsigned rfl_pad(unsigned x) { unsigned r = rfl(x); asm volatile("s_nop 4" : "+s"(r)); return r; }
DI void dma16(rsrc_t rs, unsigned voff, unsigned soff, unsigned ldsaddr) {
    asm volatile("s_mov_b32 m0, %0\n\ts_nop 0\n\tbuffer_load_dwordx4 %1, %2, %3 offen lds" ::"s"(ldsaddr), "v"(voff), "s"(rs), "s"(soff) : "memory");
}
DI void dma16_sc1(rsrc_t rs, unsigned voff, unsigned soff, unsigned ldsaddr) {
    asm volatile("s_mov_b32 m0, %0\n\ts_nop 0\n\tbuffer_load_dwordx4 %1, %2, %3 offen sc1 lds" ::"s"(ldsaddr), "v"(voff), "s"(rs), "s"(soff) : "memory");
}
#define WAIT_VM0() asm volatile("s_waitcnt vmcnt(0)" ::: "memory")
#define BAR() asm volatile("s_waitcnt lgkmcnt(0)\n\ts_barrier" ::: "memory")
#define VM0_BAR() asm volatile("s_waitcnt vmcnt(0) lgkmcnt(0)\n\ts_barrier" ::: "memory")
#define VMCNT(n) asm volatile("s_waitcnt vmcnt(%0)" ::"n"(n) : "memory")
#define VMCNT_BAR(n) asm volatile("s_waitcnt vmcnt(%0) lgkmcnt(0)\n\ts_barrier" ::"n"(n) : "memory")
#define SBAR() __builtin_amdgcn_sched_barrier(0)
#define PIN(x) asm volatile("" : "+v"(x))

template <int V> using IC = std::integral_constant<int, V>;
template <int... Is, class F> DI void static_for_(std::integer_sequence<int, Is...>, F&& f) { (f(IC<Is>{}), ...); }
template <int N, class F> DI void static_for(F&& f) { static_for_(std::make_integer_sequence<int, N>{}, f); }

DI f32x16 mfma16(u32x4 a, u32x4 b, f32x16 c) { return __builtin_amdgcn_mfma_f32_32x32x16_bf16(__builtin_bit_cast(bf16x8, a), __builtin_bit_cast(bf16x8, b), c, 0, 0, 0); }
DI f32x16 mfma_f32(float a, float b, f32x16 c) { return __builtin_amdgcn_mfma_f32_32x32x2f32(a, b, c, 0, 0, 0); }
DI unsigned cvtpk(float lo, float hi) { f32x2 v = {lo, hi}; return __builtin_bit_cast(unsigned, __builtin_convertvector(v, bf16x2)); }
DI float dot2(unsigned a, unsigned b, float c) { return __builtin_amdgcn_fdot2_f32_bf16(__builtin_bit_cast(bf16x2, a), __builtin_bit_cast(bf16x2, b), c, false); }
DI float swap_add(float x) { auto r = __builtin_amdgcn_permlane32_swap(__float_as_uint(x), __float_as_uint(x), false, false); return __uint_as_float(r[0]) + __uint_as_float(r[1]); }
DI float swap_max(float x) { auto r = __builtin_amdgcn_permlane32_swap(__float_as_uint(x), __float_as_uint(x), false, false); return fmaxf(__uint_as_float(r[0]), __uint_as_float(r[1])); }
template <int CTRL> DI float dpp_max(float x) { return fmaxf(x, __uint_as_float(__builtin_amdgcn_update_dpp(0u, __float_as_uint(x), CTRL, 0xf, 0xf, true))); }
DI float max32(float x) {
    x = dpp_max<0x128>(x); x = dpp_max<0x124>(x); x = dpp_max<0x122>(x); x = dpp_max<0x121>(x);
    auto sw = __builtin_amdgcn_permlane16_swap(__float_as_uint(x), __float_as_uint(x), false, false);
    return fmaxf(__uint_as_float(sw[0]), __uint_as_float(sw[1]));
}
template <int D2> DI u32x4 tr_frag(LAS char* p) {
    s16x4 lo = __builtin_amdgcn_ds_read_tr16_b64_v4i16((LAS s16x4*)p), hi = __builtin_amdgcn_ds_read_tr16_b64_v4i16((LAS s16x4*)(p + D2));
    u32x2 a = __builtin_bit_cast(u32x2, lo), b = __builtin_bit_cast(u32x2, hi);
    return u32x4{a[0], a[1], b[0], b[1]};
}
DI u32x4 pack8(const f32x16& v, int s) { return u32x4{cvtpk(v[8 * s], v[8 * s + 1]), cvtpk(v[8 * s + 2], v[8 * s + 3]), cvtpk(v[8 * s + 4], v[8 * s + 5]), cvtpk(v[8 * s + 6], v[8 * s + 7])}; }
DI void st16(rsrc_t rs, unsigned off, u32x4 v) { __builtin_amdgcn_raw_buffer_store_b128(v, rs, off, 0, AUX_SC1); }
DI u32x4 ld16(rsrc_t rs, unsigned voff, unsigned soff) { return __builtin_amdgcn_raw_buffer_load_b128(rs, voff, soff, AUX_SC1); }
DI float bf2f(unsigned short b) { return __uint_as_float((unsigned)b << 16); }
DI u64 ld_flag(const char* p) { return __hip_atomic_load((GAS const u64*)p, __ATOMIC_RELAXED, __HIP_MEMORY_SCOPE_AGENT); }
DI void st_flag(char* p, u64 v) { __hip_atomic_store((GAS u64*)p, v, __ATOMIC_RELAXED, __HIP_MEMORY_SCOPE_AGENT); }
DI void poll_flags(const char* p, bool use, u64 tag) {
    for (unsigned n = 0; n < SPIN_MAX; ++n) {
        const u64 v = use ? ld_flag(p) : tag;
        if (__all(v == tag)) break;
        __builtin_amdgcn_s_sleep(1);
    }
    asm volatile("" ::: "memory");
}
template <class T> DI T* opaque(T* p) { asm volatile("" : "+v"(p)); return p; }
#define LDF(p) (*(LAS u32x4*)(p))

#define STAMP(i) do {} while (0)
#define FSTAMP(i) do {} while (0)
struct Ptrs { const void* x; const void* wqkv; const void* wo; const void* w1; const void* b1; const void* w2; const void* b2; void* out; char* ws; };

constexpr unsigned L_STAT = 155648, L_CS = 156672;
DI void sum8(const u32x4& f, float& a, float& b) {
    float t0, t1, t2, t3;
    asm volatile("v_lshlrev_b32 %2, 16, %6\n\tv_and_b32 %3, 0xffff0000, %6\n\tv_lshlrev_b32 %4, 16, %7\n\tv_and_b32 %5, 0xffff0000, %7\n\t"
                 "v_add_f32 %0, %0, %2\n\tv_add_f32 %1, %1, %3\n\tv_add_f32 %0, %0, %4\n\tv_add_f32 %1, %1, %5\n\t"
                 "v_lshlrev_b32 %2, 16, %8\n\tv_and_b32 %3, 0xffff0000, %8\n\tv_lshlrev_b32 %4, 16, %9\n\tv_and_b32 %5, 0xffff0000, %9\n\t"
                 "v_add_f32 %0, %0, %2\n\tv_add_f32 %1, %1, %3\n\tv_add_f32 %0, %0, %4\n\tv_add_f32 %1, %1, %5"
                 : "+v"(a), "+v"(b), "=&v"(t0), "=&v"(t1), "=&v"(t2), "=&v"(t3) : "v"(f[0]), "v"(f[1]), "v"(f[2]), "v"(f[3]));
}
struct P1Dma { rsrc_t rqkv, rw1, rw2, rwo; unsigned vq0, vq1, vq2, vf, qsoff, qdst, fsoff, fdst, tsoff, tdst, tdummy, vt2, vto; };

template <int HALF, bool SWA, bool SWB>
DI void p1_qkv_pass(const Ptrs& P, LAS char* lds, const P1Dma& D, u32x4 (&xf)[28], unsigned tt, unsigned offA, unsigned offB, LAS char* xb0, unsigned gswb, unsigned h, bool is28,
                    f32x16& accA, f32x16& accB, float& csA, float& csB) {
    LAS char* qa[4]; LAS char* qb[4]; LAS char* qa2[4]; LAS char* qb2[4]; LAS char* xu[4];
#pragma unroll
    for (int u = 0; u < 4; ++u) {
        const unsigned ro = ((u + tt) & 3) * 3072;
        qa[u] = opaque(lds + 114688 + offA + ro); qa2[u] = opaque(lds + offA + ro);
        if (HALF == 0) { qb[u] = opaque(lds + 114688 + offB + ro); qb2[u] = opaque(lds + offB + ro); }
        xu[u] = xb0 + (((2 * ((u + tt) & 3) + h) ^ gswb) << 4);
    }
    constexpr int PF = 3;
    u32x4 wa[PF], wb[PF];
    float a0 = 0.f, a1 = 0.f, b0 = 0.f, b1 = 0.f;
    const unsigned ones = 0x3F803F80u;
    accA = f32x16{}; accB = f32x16{};
    auto rd = [&](auto SC) {
        constexpr int s = decltype(SC)::value; constexpr int o = s < 12 ? (s & ~3) * 3072 : ((s & ~3) - 12) * 3072;
        wa[s % PF] = tr_frag<768>((s < 12 ? qa[s & 3] : qa2[s & 3]) + o); if (HALF == 0) wb[s % PF] = tr_frag<768>((s < 12 ? qb[s & 3] : qb2[s & 3]) + o);
    };
    FSTAMP(16);
    rd(IC<0>{}); rd(IC<1>{}); rd(IC<2>{});
    SBAR();
    static_for<28>([&](auto SC) {
        constexpr int s = decltype(SC)::value;
        if constexpr (s == 20) FSTAMP(25);
        u32x4 ca = wa[s % PF]; const u32x4 cb = wb[s % PF];
        if (HALF == 1) { ca[0] = is28 ? ones : ca[0]; ca[1] = is28 ? ones : ca[1]; ca[2] = is28 ? ones : ca[2]; ca[3] = is28 ? ones : ca[3]; }
        accA = SWA ? mfma16(xf[s], ca, accA) : mfma16(ca, xf[s], accA);
        if constexpr (s < 6) dma16(D.rqkv, s % 3 == 0 ? D.vq0 : s % 3 == 1 ? D.vq1 : D.vq2, D.qsoff + (s / 3) * (16 * 2688), D.qdst + s * 1024);
        else if constexpr (s >= 8 && s < 16) dma16(D.rw1, D.vf, D.fsoff + (s - 8) * (4 * 3584), D.fdst + (s - 8) * 1024);
        if (HALF == 0) accB = SWB ? mfma16(xf[s], cb, accB) : mfma16(cb, xf[s], accB);
        else accB = mfma16(xf[s], xf[s], accB);
        if constexpr (s + PF < 28 && !(s + PF >= 12 && s <= 11)) rd(IC<(s + PF < 28 ? s + PF : 0)>{});
        if constexpr (s >= 4 && s < 8) {
#pragma unroll
            for (int i = 0; i < 3; ++i) { constexpr int dummy = 0; (void)dummy; const int j = 3 * (s - 4) + i; xf[16 + j] = LDF(xu[j & 3] + 128 * (j >> 2)); }
        }
        if constexpr ((s & 3) == 0) { sum8(ca, a0, a1); if (HALF == 0) sum8(cb, b0, b1); }
        SBAR();
        if constexpr (s == 12) {
            asm volatile("s_mov_b32 m0, %0\n\ts_nop 0\n\tbuffer_load_dword %1, %2, 0 offen lds\n\tbuffer_load_dword %3, %4, 0 offen lds" ::"s"(D.tdummy), "v"(D.vt2), "s"(D.rw2), "v"(D.vto), "s"(D.rwo) : "memory");
        }
        if constexpr (s == 3) { FSTAMP(17); VMCNT_BAR(4); FSTAMP(18); SBAR(); }
        if constexpr (s == 7) { FSTAMP(19); BAR(); FSTAMP(20); SBAR(); }
        if constexpr (s == 11) {
            FSTAMP(21); VMCNT_BAR(4); FSTAMP(24);
            rd(IC<12>{}); rd(IC<13>{}); rd(IC<14>{});
            SBAR();
        }
    });
    csA = a0 + a1; csB = b0 + b1;
}
template <bool TAIL>
DI void p1_ffn_pass(const Ptrs& P, LAS char* lds, const P1Dma& D, const u32x4 (&xf)[28], unsigned tt, unsigned off, f32x16& acc, float& cs, char* flag, u64 tag) {
    LAS char* q[4]; LAS char* q2[4];
#pragma unroll
    for (int u = 0; u < 4; ++u) { const unsigned ro = off + ((u + tt) & 3) * 4096; q[u] = opaque(lds + 49152 + ro); q2[u] = opaque(lds + ro); }
    constexpr int PF = 4;
    u32x4 wf[PF];
    float c0 = 0.f, c1 = 0.f;
    acc = f32x16{};
    auto rd = [&](auto SC) { constexpr int s = decltype(SC)::value; wf[s % PF] = tr_frag<1024>(s < 16 ? q[s & 3] + (s & ~3) * 4096 : q2[s & 3] + ((s & ~3) - 16) * 4096); };
    rd(IC<0>{}); rd(IC<1>{}); rd(IC<2>{}); rd(IC<3>{});
    SBAR();
    static_for<28>([&](auto SC) {
        constexpr int s = decltype(SC)::value;
        const u32x4 cur = wf[s % PF];
        acc = mfma16(cur, xf[s], acc);
        if constexpr (TAIL && s < 6) dma16(D.rw1, D.vf, D.tsoff + s * (4 * 3584), D.tdst + s * 1024);
        if constexpr (s + PF < 28 && !(TAIL && s + PF >= 16 && s <= 15)) rd(IC<(s + PF < 28 ? s + PF : 0)>{});
        if constexpr ((s & 3) == 0) sum8(cur, c0, c1);
        SBAR();
        if constexpr (TAIL && s == 15) {
            FSTAMP(22);
            VMCNT_BAR(0);
            FSTAMP(23);
            if (threadIdx.x == 0) st_flag(flag, tag);
            rd(IC<16>{}); rd(IC<17>{}); rd(IC<18>{}); rd(IC<19>{});
            SBAR();
        }
    });
    cs = c0 + c1;
}
DI float p1_cs(LAS float* csb, unsigned l31) {
    float v[8];
#pragma unroll
    for (int i = 0; i < 8; ++i) v[i] = csb[i * 32 + l31];
    return ((v[0] + v[1]) + (v[2] + v[3])) + ((v[4] + v[5]) + (v[6] + v[7]));
}
struct P1Ctx { rsrc_t rws; char* ws; unsigned lane, l31, h, tile; float mu, sig, rstd; };
DI void p1_epi_ffn(const P1Ctx& C, f32x16 acc, float cs, float bias, unsigned ft) {
    acc = mfma_f32(C.h ? bias : cs, C.h ? C.sig : -C.mu, acc);
#pragma unroll
    for (int i = 0; i < 16; i += 2) {
        const f32x2 xv = f32x2{acc[i], acc[i + 1]} * C.rstd, t2 = xv * xv;
        const f32x2 pl = __builtin_elementwise_fma(t2, f32x2{0.1029432397f, 0.1029432397f}, f32x2{2.302208198f, 2.302208198f}), z = xv * pl;
        const f32x2 d = f32x2{__builtin_amdgcn_exp2f(-z[0]), __builtin_amdgcn_exp2f(-z[1])} + 1.f;
        const f32x2 y = xv * f32x2{__builtin_amdgcn_rcpf(d[0]), __builtin_amdgcn_rcpf(d[1])};
        acc[i] = y[0]; acc[i + 1] = y[1];
    }
    const unsigned off = WS_H + (C.tile * 112 + 2 * ft) * 1024 + C.lane * 16;
    st16(C.rws, off, pack8(acc, 0)); st16(C.rws, off + 1024, pack8(acc, 1));
}
DI void p1_epi_qkv(const P1Ctx& C, f32x16 acc, float cs, unsigned type, unsigned head) {
    const unsigned h = C.h, l31 = C.l31;
    if (type == 2) {
        acc = mfma_f32(h ? 0.f : -C.mu, h ? 0.f : cs, acc);
        f32x16 R = {}; R = mfma_f32(h ? 0.f : C.rstd, h ? 0.f : 1.f, R);
        const float one = l31 == 28 ? 1.f : 0.f;
#pragma unroll
        for (int i = 0; i < 16; ++i) acc[i] = l31 < 28 ? acc[i] * R[i] : one;
    } else {
        acc = mfma_f32(h ? 0.f : cs, h ? 0.f : -C.mu, acc);
        const float sc = type == 0 ? C.rstd * C2 : C.rstd;
#pragma unroll
        for (int i = 0; i < 16; ++i) acc[i] *= sc;
        if (h) { acc[12] = type == 1 ? 1.f : 0.f; acc[13] = 0.f; acc[14] = 0.f; acc[15] = 0.f; }
        if (type == 1) {
            float n2 = 0.f, n3 = 0.f;
#pragma unroll
            for (int i = 0; i < 12; i += 2) { n2 = __builtin_fmaf(acc[i], acc[i], n2); n3 = __builtin_fmaf(acc[i + 1], acc[i + 1], n3); }
            if (!h) {
#pragma unroll
                for (int i = 12; i < 16; i += 2) { n2 = __builtin_fmaf(acc[i], acc[i], n2); n3 = __builtin_fmaf(acc[i + 1], acc[i + 1], n3); }
            }
            n2 = max32(swap_add(n2 + n3));
            if (C.lane == 0) __hip_atomic_store((GAS float*)(C.ws + WS_KN2) + head * 64 + C.tile, n2, __ATOMIC_RELAXED, __HIP_MEMORY_SCOPE_AGENT);
        }
    }
    const unsigned off = WS_Q + type * (2u << 20) + (head * 64 + C.tile) * 2048 + C.lane * 16;
    st16(C.rws, off, pack8(acc, 0)); st16(C.rws, off + 1024, pack8(acc, 1));
}

DI u64 make_tag(u64 e) {
    e = ((u64)rfl((unsigned)(e >> 32)) << 32) | rfl((unsigned)e);
    u64 tag = (e + 0x9E3779B97F4A7C15ull); tag = (tag ^ (tag >> 30)) * 0xBF58476D1CE4E5B9ull; tag = (tag ^ (tag >> 27)) * 0x94D049BB133111EBull; tag ^= tag >> 31;
    return tag;
}
DI u64 phase1(const Ptrs& P, LAS char* lds, int b) {
    const unsigned tid = threadIdx.x, lane = tid & 63, l31 = lane & 31, h = lane >> 5;
    const unsigned w = rfl(tid >> 6), tt = w & 3, half = w >> 2;
    const unsigned g = b & 7, r = b >> 3, tb = 4 * (g >> 1) + (r & 3), cg = 8 * (g & 1) + (r >> 2);
    const unsigned lds0 = (unsigned)(size_t)lds;
    const rsrc_t rx = mk_rsrc(P.x, SEQ * DM * 2);
    P1Dma D;
    D.rqkv = mk_rsrc(P.wqkv, DM * 3 * DM * 2);
    const unsigned cb = 168 * cg, cb16 = cb & ~15u, dl = cb & 15u;
    FSTAMP(13);
    {
        const unsigned rowa = 2 * w + h;
        const unsigned va = rowa * 896 + ((l31 ^ (rowa & 15)) << 4), sa = tb * 128 * 896;
#pragma unroll
        for (unsigned i = 0; i < 8; ++i) dma16(rx, va, sa + i * (16 * 896), lds0 + (w + 8 * i) * 1024);
    }
    {
        auto vq = [&](unsigned p) { const unsigned row = p / 12, ch = p - row * 12; return ch < 11 ? row * 2688 + cb16 + ch * 16 : OOB; };
        D.vq0 = vq(lane); D.vq1 = vq(64 + lane); D.vq2 = vq(128 + lane);
    }
    {
        const unsigned so = w * (16 * 2688), ds = lds0 + 114688 + w * 3072;
        dma16(D.rqkv, D.vq0, so, ds); dma16(D.rqkv, D.vq1, so, ds + 1024); dma16(D.rqkv, D.vq2, so, ds + 2048);
        if (w < 4) { dma16(D.rqkv, D.vq0, so + 8 * (16 * 2688), ds + 8 * 3072); dma16(D.rqkv, D.vq1, so + 8 * (16 * 2688), ds + 8 * 3072 + 1024); dma16(D.rqkv, D.vq2, so + 8 * (16 * 2688), ds + 8 * 3072 + 2048); }
    }
    const unsigned ntile = ((cg ^ (cg >> 1)) & 1) ? 3 : 4, fstart = 7 * (cg >> 1) + ((cg & 1) ? (((cg >> 1) & 1) ? 3 : 4) : 0);
    D.rw1 = mk_rsrc(P.w1, DM * FF * 2);
    { const unsigned rq = lane >> 4, sl = ((lane >> 2) & 3) ^ rq; D.vf = sl < ntile ? rq * 3584 + (fstart + sl) * 64 + (lane & 3) * 16 : OOB; }
    D.qsoff = (12 + 2 * w) * (16 * 2688); D.qdst = lds0 + 2 * w * 3072;
    D.fsoff = 2 * w * (16 * 3584); D.fdst = lds0 + 49152 + 2 * w * 4096;
    D.tsoff = (256 + 24 * w) * 3584; D.tdst = lds0 + 6 * w * 1024;
    D.rw2 = mk_rsrc(P.w2, FF * DM * 2); D.rwo = mk_rsrc(P.wo, DM * DM * 2); D.tdummy = lds0 + 151552 + w * 256;
    { const unsigned i = w * 8 + (lane & 7); D.vt2 = (lane < 8 && i < 49) ? (b * 49 + i) * 128 : OOB; D.vto = (lane < 8 && i < 13) ? (b * 13 + i) * 128 : OOB; }
    const u64 eraw = ld_flag(P.ws + WS_EPOCH);
    const unsigned short b1a = ((const unsigned short*)P.b1)[(fstart + 2 * half) * 32 + l31];
    const unsigned short b1b = ((const unsigned short*)P.b1)[(2 * half + 1 < ntile ? fstart + 2 * half + 1 : fstart) * 32 + l31];
#pragma unroll
    for (unsigned i = 0; i < 6; ++i) {
        const unsigned p = 64 * i + lane, rr = p / 24, c = p - rr * 24, row = 16 * w + rr;
        dma16(rx, row * 896 + 512 + ((c ^ ((row >> 1) & 7)) << 4), tb * 128 * 896, lds0 + 65536 + (6 * w + i) * 1024);
    }
    FSTAMP(14);
    VMCNT_BAR(6);
    STAMP(1);
    u32x4 xf[28];
    {
        LAS char* xa = lds + (tt * 32 + l31) * 512;
#pragma unroll
        for (int u = 0; u < 4; ++u) {
            const unsigned c = 2 * ((u + tt) & 3) + h;
            LAS char* x0 = xa + ((c ^ (l31 & 15)) << 4); LAS char* x1 = xa + (((c + 8) ^ (l31 & 15)) << 4);
            xf[u] = LDF(x0); xf[4 + u] = LDF(x1); xf[8 + u] = LDF(x0 + 256); xf[12 + u] = LDF(x1 + 256);
        }
    }
    BAR();
    STAMP(2);
    const u64 tag = make_tag(eraw);
    const rsrc_t rws = mk_rsrc(P.ws, WS_BYTES);
    LAS float* stat = (LAS float*)(lds + L_STAT);
    LAS float* csb = (LAS float*)(lds + L_CS);
    P1Ctx C{rws, P.ws, lane, l31, h, tb * 4 + tt, 0.f, 0.f, 0.f};
    const unsigned G = lane >> 4, q4 = (lane & 15) >> 2;
    const unsigned trq = (8 * (G >> 1) + q4) * 192 + dl + (G & 1) * 32 + (lane & 3) * 8;
    const unsigned trf = (8 * (G >> 1) + q4) * 256 + (G & 1) * 32 + (lane & 3) * 8;
    f32x16 accA, accB; float csA, csB;
    const unsigned tA = 3 * cg + 2 * half, tB = 3 * cg + 1, tyA = tA >> 4, tyB = tB >> 4;
    LAS char* xb0 = lds + 65536 + (tt * 32 + l31) * 384; const unsigned gswb = (l31 >> 1) & 7;
    const bool m28 = l31 == 28;
    if (half == 0) {
        if (tyA == 2) p1_qkv_pass<0, true, true>(P, lds, D, xf, tt, trq, trq + 56, xb0, gswb, h, m28, accA, accB, csA, csB);
        else if (tyB == 2) p1_qkv_pass<0, false, true>(P, lds, D, xf, tt, trq, trq + 56, xb0, gswb, h, m28, accA, accB, csA, csB);
        else p1_qkv_pass<0, false, false>(P, lds, D, xf, tt, trq, trq + 56, xb0, gswb, h, m28, accA, accB, csA, csB);
        csb[0 * 256 + tt * 64 + lane] = csA; csb[1 * 256 + tt * 64 + lane] = csB;
    } else {
        float a1;
        if (tyA == 2) {
            p1_qkv_pass<1, true, false>(P, lds, D, xf, tt, trq + 112, trq, xb0, gswb, h, m28, accA, accB, csA, csB);
            if (l31 == 28) {
#pragma unroll
                for (int i = 0; i < 16; ++i) stat[(tt * 32 + (i & 3) + 8 * (i >> 2) + 4 * h) * 2] = accA[i];
            }
            asm volatile("s_waitcnt lgkmcnt(0)" ::: "memory");
            a1 = stat[(tt * 32 + l31) * 2];
        } else {
            p1_qkv_pass<1, false, false>(P, lds, D, xf, tt, trq + 112, trq, xb0, gswb, h, m28, accA, accB, csA, csB);
            auto sw = __builtin_amdgcn_permlane32_swap(__float_as_uint(accA[12]), __float_as_uint(accA[12]), false, false);
            a1 = __uint_as_float(sw[1]);
        }
        csb[2 * 256 + tt * 64 + lane] = csA;
        const unsigned ri = (l31 & 3) + 4 * (l31 >> 3);
        float dg = 0.f;
#pragma unroll
        for (int i = 0; i < 16; ++i) dg = ri == (unsigned)i ? accB[i] : dg;
        const float a2 = swap_add(h == ((l31 >> 2) & 1) ? dg : 0.f);
        const float m = a1 * (1.f / DM), var = fmaxf(a2 * (1.f / DM) - m * m, 0.f) + LN_EPS;
        if (!h) *(LAS f32x2*)(stat + (tt * 32 + l31) * 2) = f32x2{m, __builtin_amdgcn_rsqf(var)};
    }
    FSTAMP(2);
    BAR();
    STAMP(3);
    { const f32x2 st = *(LAS f32x2*)(stat + (tt * 32 + l31) * 2); C.mu = st[0]; C.rstd = st[1]; C.sig = __builtin_amdgcn_rcpf(st[1]); }
    {
        const float c0 = p1_cs(csb + 2 * half * 256, l31), c1 = p1_cs(csb + 256, l31);
        p1_epi_qkv(C, accA, c0, tyA, tA & 15);
        if (half == 0) p1_epi_qkv(C, accB, c1, tyB, tB & 15);
    }
    FSTAMP(5);
    STAMP(4);
    if (half == 0) __builtin_amdgcn_s_setprio(1);
    p1_ffn_pass<true>(P, lds, D, xf, tt, trf + 64 * ((2 * half) ^ q4), accA, csA, P.ws + WS_FQKV + (cg * 16 + tb) * 64, tag);
    csb[(3 + 2 * half) * 256 + tt * 64 + lane] = csA;
    FSTAMP(9);
    BAR();
    STAMP(5);
    if (half == 0) {
        p1_epi_ffn(C, accA, p1_cs(csb + 3 * 256, l31), bf2f(b1a), fstart);
        p1_ffn_pass<false>(P, lds, D, xf, tt, trf + 64 * (1 ^ q4), accA, csA, nullptr, 0);
        csb[4 * 256 + tt * 64 + lane] = csA;
        BAR();
        p1_epi_ffn(C, accA, p1_cs(csb + 4 * 256, l31), bf2f(b1b), fstart + 1);
    } else {
        if (ntile == 4) { p1_ffn_pass<false>(P, lds, D, xf, tt, trf + 64 * (3 ^ q4), accB, csB, nullptr, 0); csb[6 * 256 + tt * 64 + lane] = csB; }
        p1_epi_ffn(C, accA, p1_cs(csb + 5 * 256, l31), bf2f(b1a), fstart + 2);
        BAR();
        if (ntile == 4) p1_epi_ffn(C, accB, p1_cs(csb + 6 * 256, l31), bf2f(b1b), fstart + 3);
    }
    FSTAMP(12);
    __builtin_amdgcn_s_setprio(0);
    STAMP(6);
    return tag;
}

constexpr unsigned p3_region(int sb) { return sb % 3 == 0 ? 65536u : sb % 3 == 1 ? 114688u : 0u; }
struct P3W { rsrc_t rw2, rwo; unsigned vw, vo0, vo1, soff, dst, hd0; };
template <int SB, int I> DI void p3_dma(const P3W& W) {
    constexpr int j = 6 * SB + I;
    if constexpr (j < 28) dma16(W.rw2, W.vw, W.soff + j * (16 * DM * 2), W.dst + p3_region(SB) + I * 2048);
    else {
        dma16(W.rwo, ((j - 28) & 1) ? W.vo1 : W.vo0, W.hd0 + ((j - 28) >> 1) * (28 * DM * 2), W.dst + p3_region(SB) + I * 2048);
    }
}
constexpr int att_aflo(int T) { return (!XA || T < 6) ? 0 : (T - 6) * 28 / 26; }
constexpr int att_kv(int T) { return (XL && (T & 1) == 0 && (T >> 1) + 3 < 16) ? 2 : 0; }
constexpr int att_all(int T0, int T1) { int n = 0; for (int T = T0; T < T1; ++T) n += att_kv(T) + (att_aflo(T + 1) - att_aflo(T)); return n; }
#define EXP4(S, i) do { S[i] = __builtin_amdgcn_exp2f(S[i]); S[i + 1] = __builtin_amdgcn_exp2f(S[i + 1]); S[i + 2] = __builtin_amdgcn_exp2f(S[i + 2]); S[i + 3] = __builtin_amdgcn_exp2f(S[i + 3]); } while (0)
template <int KH>
DI void att_fast(LAS char* lds, rsrc_t rws, unsigned lane, unsigned w, unsigned src, unsigned wdst, unsigned afsrc, u32x4 qf0, u32x4 qf1, f32x16& o, u32x4 (&af)[28], const char* hflag, u64 tag) {
    LAS char* kvb = lds + KH * 8192 + lane * 16;
    const unsigned vo = lane * 16, vo1 = lane * 16 + 1024;
    src = rfl_pad(src); wdst = rfl_pad(wdst); afsrc = rfl_pad(afsrc);
    const f32x16 zero = {};
    f32x16 sc[2];
    u32x4 k0 = LDF(kvb), k1 = LDF(kvb + 1024), v0 = LDF(kvb + 2048), v1 = LDF(kvb + 3072), p1 = {0u, 0u, 0u, 0u}, v1p = {0u, 0u, 0u, 0u};
    sc[0] = mfma16(k0, qf0, zero); sc[0] = mfma16(k1, qf1, sc[0]);
    k0 = LDF(kvb + 4096); k1 = LDF(kvb + 4096 + 1024);
    static_for<32>([&](auto TC) {
        constexpr int T = decltype(TC)::value, c = T >> 1;
        f32x16& S = sc[T & 1]; f32x16& SN = sc[(T + 1) & 1];
        constexpr int o1 = (((T + 1) >> 1) % 4) * 16384 + ((T + 1) & 1) * 4096, o2 = (((T + 2) >> 1) % 4) * 16384 + ((T + 2) & 1) * 4096;
        u32x4 p0, k0n, k1n, v0n, v1n;
        auto chunk_barrier = [&](auto OPS) {
            constexpr int younger = decltype(OPS)::value;
            if constexpr (T == 5 && KH == 0) { if (w == 0) { PIN(p1); PIN(o); poll_flags(hflag, lane < 16, tag); } }
            if (XB) asm volatile("s_waitcnt vmcnt(%0) lgkmcnt(%1)\n\ts_barrier" ::"n"(younger), "n"(KH == 0 ? 4 : 2) : "memory");
        };
        S[0] = __builtin_amdgcn_exp2f(S[0]); S[1] = __builtin_amdgcn_exp2f(S[1]);
        if (T > 0) o = mfma16(v1p, p1, o);
        S[2] = __builtin_amdgcn_exp2f(S[2]); S[3] = __builtin_amdgcn_exp2f(S[3]);
        if (T + 2 < 32) { k0n = LDF(kvb + o2); k1n = LDF(kvb + o2 + 1024); }
        p0[0] = cvtpk(S[0], S[1]); p0[1] = cvtpk(S[2], S[3]); SBAR();
        if (T + 1 < 32) SN = mfma16(k0, qf0, zero);
        EXP4(S, 4);
        if constexpr (att_kv(T) != 0) { dma16_sc1(rws, vo, src + (c + 3) * 4096, wdst + ((c + 3) % 4) * 16384); dma16_sc1(rws, vo1, src + (c + 3) * 4096, wdst + ((c + 3) % 4) * 16384 + 1024); }
        static_for<att_aflo(T + 1) - att_aflo(T)>([&](auto JC) { constexpr int j = att_aflo(T) + decltype(JC)::value; af[j] = ld16(rws, vo + (j & 3) * 1024, afsrc + (j >> 2) * 4096); });
        p0[2] = cvtpk(S[4], S[5]); p0[3] = cvtpk(S[6], S[7]); SBAR();
        constexpr int younger = c >= 1 ? att_all(2 * c - 2, T + 1) - att_kv(2 * c - 2) : att_all(0, T + 1);
        if constexpr (KH == 1 && (T & 1) == 1 && T < 31) chunk_barrier(IC<younger>{});
        if (T + 1 < 32) SN = mfma16(k1, qf1, SN);
        EXP4(S, 8);
        if (T + 1 < 32) { v0n = LDF(kvb + o1 + 2048); v1n = LDF(kvb + o1 + 3072); }
        p1[0] = cvtpk(S[8], S[9]); p1[1] = cvtpk(S[10], S[11]); SBAR();
        o = mfma16(v0, p0, o);
        EXP4(S, 12);
        p1[2] = cvtpk(S[12], S[13]); p1[3] = cvtpk(S[14], S[15]); SBAR();
        v1p = v1;
        if (T + 2 < 32) { k0 = k0n; k1 = k1n; }
        if (T + 1 < 32) { v0 = v0n; v1 = v1n; }
        if constexpr (KH == 0 && (T & 1) == 1 && T < 31) chunk_barrier(IC<younger>{});
    });
    o = mfma16(v1p, p1, o);
}

DI void phase23(const Ptrs& P, LAS char* lds, int b, u64 tag) {
    const unsigned tid = threadIdx.x, lane = tid & 63, l31 = lane & 31, h = lane >> 5;
    const unsigned w = rfl(tid >> 6), qt = w & 3, kh = w >> 2;
    const unsigned g = b & 7, r = b >> 3, head = 2 * g + (r >> 4), qb = r & 15, tile = qb * 4 + qt;
    const unsigned lds0 = (unsigned)(size_t)lds;
    const rsrc_t rws = mk_rsrc(P.ws, WS_BYTES);
    const bool p3 = r < 28;
    const unsigned tt3 = w & 1, q3 = w >> 1, tb3 = 4 * g + (r & 3), cg3 = p3 ? r >> 2 : 0, tile3 = tb3 * 2 + tt3;
    P3W W3;
    {
        const unsigned vcol = cg3 * 128 + (((lane >> 2) & 1) ^ ((lane >> 4) & 1)) * 64 + (lane & 3) * 16, rw = (w & 1) * 8 + (lane >> 3);
        W3.rw2 = mk_rsrc(P.w2, FF * DM * 2); W3.rwo = mk_rsrc(P.wo, DM * DM * 2);
        W3.vw = rw * (DM * 2) + vcol; W3.vo0 = W3.vw; W3.vo1 = W3.vw + 16 * (DM * 2);
        W3.soff = 28 * (w >> 1) * (16 * DM * 2); W3.hd0 = 4 * (w >> 1) * (28 * DM * 2);
        W3.dst = lds0 + (w >> 1) * 6 * 2048 + (w & 1) * 1024;
        W3.soff = rfl_pad(W3.soff); W3.hd0 = rfl_pad(W3.hd0); W3.dst = rfl_pad(W3.dst);
    }
    BAR();
    static_for<12>([&](auto TC) { constexpr int T = decltype(TC)::value; p3_dma<T / 6, T % 6>(W3); });
    if (w == 0) {
        const unsigned cq = head / 3, ck = (16 + head) / 3, cv = (32 + head) / 3;
        const unsigned idx = lane < 16 ? ck * 16 + lane : lane < 32 ? cv * 16 + (lane - 16) : cq * 16 + qb;
        poll_flags(P.ws + WS_FQKV + idx * 64, lane <= 32, tag);
    }
    BAR();
    STAMP(7);
    const unsigned src = rfl_pad(((w & 1) ? WS_V : WS_K) + (head * 64 + kh * 32 + ((w >> 1) & 1)) * 2048);
    const unsigned wdst = rfl_pad(lds0 + w * 2048);
#define STAGE(c) do { dma16_sc1(rws, lane * 16, src + (c) * 4096, wdst + ((c) % 4) * 16384); dma16_sc1(rws, lane * 16 + 1024, src + (c) * 4096, wdst + ((c) % 4) * 16384 + 1024); } while (0)
    STAGE(0); STAGE(1);
    u32x4 qf0 = ld16(rws, lane * 16, WS_Q + (head * 64 + tile) * 2048), qf1 = ld16(rws, lane * 16 + 1024, WS_Q + (head * 64 + tile) * 2048);
    float kn2 = __hip_atomic_load((GAS float*)(P.ws + WS_KN2) + head * 64 + lane, __ATOMIC_RELAXED, __HIP_MEMORY_SCOPE_AGENT);
    kn2 = swap_max(max32(kn2));
    float qn2 = 0.f;
#pragma unroll
    for (int e = 0; e < 4; ++e) { qn2 = dot2(qf0[e], qf0[e], qn2); qn2 = dot2(qf1[e], qf1[e], qn2); }
    qn2 = swap_add(qn2);
    const float bound = __builtin_amdgcn_sqrtf(qn2 * kn2) * 1.01f + 0.01f;
    const bool safe = __all(bound <= SAFE_BOUND);
    float mrun;
    if (safe) {
        const unsigned nb = cvtpk(-bound, 0.f) & 0xffffu;
        if (h) qf1[2] = (qf1[2] & 0xffff0000u) | nb;
        mrun = __uint_as_float((nb & 0x7fffu) << 16);
    } else mrun = -INFINITY;
    PIN(qf1); PIN(mrun);
    WAIT_VM0();
    STAGE(2);
    BAR();
    if (tid == 0) st_flag(P.ws + WS_FH + ((4 * (g >> 1) + (r & 3)) * 16 + 8 * (g & 1) + (r >> 2)) * 64, tag);
    STAMP(8);
    f32x16 o = {};
    u32x4 af[28];
    const unsigned afsrc = WS_H + (tile3 * 112 + 28 * q3) * 1024;
    const char* hflag = P.ws + WS_FH + ((tb3 >> 1) * 16 + (lane & 15)) * 64;
    if (safe) {
        if (kh == 0) att_fast<0>(lds, rws, lane, w, src, wdst, afsrc, qf0, qf1, o, af, hflag, tag);
        else att_fast<1>(lds, rws, lane, w, src, wdst, afsrc, qf0, qf1, o, af, hflag, tag);
    } else {
        const f32x16 zero = {};
        LAS char* kvb = lds + kh * 8192 + lane * 16;
#pragma unroll 1
        for (unsigned c = 0; c < 16; ++c) {
            if (c + 3 < 16) STAGE(c + 3);
#pragma unroll 1
            for (int t = 0; t < 2; ++t) {
                LAS char* kv = kvb + (c & 3) * 16384 + t * 4096;
                const u32x4 k0 = LDF(kv), k1 = LDF(kv + 1024), v0 = LDF(kv + 2048), v1 = LDF(kv + 3072);
                f32x16 s = mfma16(k0, qf0, zero); s = mfma16(k1, qf1, s);
                float m = s[0];
#pragma unroll
                for (int i = 1; i < 16; ++i) m = fmaxf(m, s[i]);
                m = fmaxf(swap_max(m), mrun);
                const float f = __builtin_amdgcn_exp2f(mrun - m);
                mrun = m;
#pragma unroll
                for (int i = 0; i < 16; ++i) { s[i] = __builtin_amdgcn_exp2f(s[i] - m); o[i] *= f; }
                o = mfma16(v0, pack8(s, 0), o); o = mfma16(v1, pack8(s, 1), o);
            }
            if (c == 2 && w == 0) poll_flags(hflag, lane < 16, tag);
            if (c < 15) VM0_BAR();
        }
#pragma unroll
        for (int j = 0; j < 28; ++j) af[j] = ld16(rws, lane * 16, afsrc + j * 1024);
    }
    BAR();
    STAMP(9);
    LAS float* ex = (LAS float*)(lds + 49152 + qt * 4096);
    if (kh == 1) {
        if (h) o[15] = mrun;
#pragma unroll
        for (int i = 0; i < 16; ++i) ex[i * 64 + lane] = o[i];
    }
    BAR();
    if (kh == 0) {
        const float m2 = ex[15 * 64 + 32 + l31], m = fmaxf(mrun, m2), f1 = __builtin_amdgcn_exp2f(mrun - m), f2 = __builtin_amdgcn_exp2f(m2 - m);
#pragma unroll
        for (int i = 0; i < 16; ++i) o[i] = o[i] * f1 + ex[i * 64 + lane] * f2;
        auto sw = __builtin_amdgcn_permlane32_swap(__float_as_uint(o[12]), __float_as_uint(o[12]), false, false);
        const float l = __uint_as_float(sw[1]);
        const float il = __builtin_amdgcn_rcpf(l);
#pragma unroll
        for (int i = 0; i < 16; ++i) o[i] *= il;
        if (h) { o[12] = 0.f; o[13] = 0.f; o[14] = 0.f; o[15] = 0.f; }
        const unsigned off = WS_O + (tile * 16 + head) * 2048 + lane * 16;
        st16(rws, off, pack8(o, 0)); st16(rws, off + 1024, pack8(o, 1));
    }
    STAMP(10);
    if (!p3) {
        VM0_BAR();
        if (tid == 0) st_flag(P.ws + WS_FO + (qb * 16 + head) * 64, tag);
        if (b == 255 && w == 0) {
#pragma unroll 1
            for (int i = 0; i < 4; ++i) poll_flags(P.ws + WS_FQKV + (i * 64 + lane) * 64, true, tag);
            if (lane == 0) st_flag(P.ws + WS_EPOCH, tag);
        }
        return;
    }
    const unsigned q4 = (lane & 15) >> 2, trb = (4 * (lane >> 5) + q4) * 128 + ((lane >> 4) & 1) * 32 + (lane & 3) * 8 + q3 * 12 * 1024;
    const unsigned tro0 = trb + ((q4 >> 1) & 1) * 64, tro1 = trb + (((q4 >> 1) & 1) ^ 1) * 64;
    const unsigned tok = tile3 * 32 + l31, n0 = cg3 * 64 + q3 * 16;
    u32x2 xr[2], br[2];
#pragma unroll
    for (int gq = 0; gq < 2; ++gq) {
        const unsigned n = n0 + 8 * gq + 4 * h;
        xr[gq] = *(const u32x2*)((const char*)P.x + (tok * DM + n) * 2); br[gq] = *(const u32x2*)((const char*)P.b2 + n * 2);
    }
    f32x16 acc0 = {}, acc1 = {};
    u32x4 ao[8];
    static_for<6>([&](auto SC) {
        constexpr int sb = decltype(SC)::value;
        LAS char* wp0 = opaque(lds + p3_region(sb) + tro0); LAS char* wp1 = opaque(lds + p3_region(sb) + tro1);
        u32x4 w0[3], w1[3];
#pragma unroll
        for (int i = 0; i < 3; ++i) { w0[i] = tr_frag<1024>(wp0 + i * 2048); w1[i] = tr_frag<1024>(wp1 + i * 2048); }
        if (sb == 3) {
#pragma unroll
            for (int j = 0; j < 8; ++j) ao[j] = ld16(rws, lane * 16 + (j & 3) * 1024, WS_O + (tile3 * 32 + 8 * q3) * 1024 + (j >> 2) * 4096);
        }
        SBAR();
        static_for<6>([&](auto IC_) {
            constexpr int i = decltype(IC_)::value, j = 6 * sb + i;
            const u32x4 a = j < 28 ? af[j < 28 ? j : 0] : ao[j >= 28 ? j - 28 : 0];
            acc0 = mfma16(w0[i % 3], a, acc0);
            if constexpr (sb + 2 < 6) p3_dma<(sb + 2 < 6 ? sb + 2 : 0), i>(W3);
            acc1 = mfma16(w1[i % 3], a, acc1);
            if (i + 3 < 6) { w0[i % 3] = tr_frag<1024>(wp0 + (i + 3) * 2048); w1[i % 3] = tr_frag<1024>(wp1 + (i + 3) * 2048); }
            SBAR();
        });
        if (sb == 2 && w == 0) poll_flags(P.ws + WS_FO + ((tb3 >> 1) * 16 + (lane & 15)) * 64, lane < 16, tag);
        if (sb < 4) asm volatile("s_waitcnt vmcnt(6) lgkmcnt(0)\n\ts_barrier" ::: "memory");
        else if (sb == 4) VM0_BAR();
        if (sb == 0 && tid == 0) st_flag(P.ws + WS_FO + (qb * 16 + head) * 64, tag);
        if (sb == 2) STAMP(11);
        if (sb == 4) STAMP(12);
    });
    BAR();
    STAMP(13);
    LAS float* rx = (LAS float*)(lds + 65536);
    float v[8];
    auto scatter = [&](auto QC) {
        constexpr int Q = decltype(QC)::value;
#pragma unroll
        for (int d = 0; d < 4; ++d) {
            if (d != Q) {
#pragma unroll
                for (int i = 0; i < 8; ++i) rx[(((tt3 * 4 + d) * 4 + Q) * 8 + i) * 64 + lane] = (d >> 1) ? acc1[8 * (d & 1) + i] : acc0[8 * (d & 1) + i];
            } else {
#pragma unroll
                for (int i = 0; i < 8; ++i) v[i] = (d >> 1) ? acc1[8 * (d & 1) + i] : acc0[8 * (d & 1) + i];
            }
        }
    };
    if (q3 == 0) scatter(IC<0>{}); else if (q3 == 1) scatter(IC<1>{}); else if (q3 == 2) scatter(IC<2>{}); else scatter(IC<3>{});
    BAR();
#pragma unroll
    for (int s = 1; s < 4; ++s) {
        const unsigned sq = (q3 + s) & 3;
#pragma unroll
        for (int i = 0; i < 8; ++i) v[i] += rx[(((tt3 * 4 + q3) * 4 + sq) * 8 + i) * 64 + lane];
    }
#pragma unroll
    for (int gq = 0; gq < 2; ++gq) {
        const unsigned n = n0 + 8 * gq + 4 * h;
        const float y0 = v[4 * gq] + __uint_as_float(xr[gq][0] << 16) + __uint_as_float(br[gq][0] << 16), y1 = v[4 * gq + 1] + __uint_as_float(xr[gq][0] & 0xffff0000u) + __uint_as_float(br[gq][0] & 0xffff0000u);
        const float y2 = v[4 * gq + 2] + __uint_as_float(xr[gq][1] << 16) + __uint_as_float(br[gq][1] << 16), y3 = v[4 * gq + 3] + __uint_as_float(xr[gq][1] & 0xffff0000u) + __uint_as_float(br[gq][1] & 0xffff0000u);
        *(u32x2*)((char*)P.out + (tok * DM + n) * 2) = u32x2{cvtpk(y0, y1), cvtpk(y2, y3)};
    }
    STAMP(14);
}

__global__ __launch_bounds__(512, 2) void fused_kernel(const void* x, const void* wqkv, const void* wo, const void* w1, const void* b1, const void* w2, const void* b2, void* out, char* ws) {
    extern __shared__ __attribute__((aligned(16))) char lds_raw[];
    LAS char* lds = (LAS char*)lds_raw;
    const Ptrs P{x, wqkv, wo, w1, b1, w2, b2, out, ws};
    STAMP(0);
    const u64 tag = phase1(P, lds, blockIdx.x);
    phase23(P, lds, blockIdx.x, tag);
}

extern "C" void kernel_launch(void* const* d_in, const int* in_sizes, int n_in, void* d_out, int out_size, void* d_ws, size_t ws_size, hipStream_t stream) {
    (void)in_sizes; (void)n_in; (void)out_size; (void)ws_size;
    (void)hipFuncSetAttribute((const void*)fused_kernel, hipFuncAttributeMaxDynamicSharedMemorySize, LDS_BYTES);
    fused_kernel<<<256, 512, LDS_BYTES, stream>>>(d_in[0], d_in[1], d_in[2], d_in[4], d_in[5], d_in[6], d_in[7], d_out, (char*)d_ws);
}
```

```cpp
#include <hip/hip_runtime.h>
#include <hip/hip_bf16.h>
#include <stdint.h>
#include <type_traits>
#include <utility>

#define DI __device__ __forceinline__
#define LAS __attribute__((address_space(3)))
#define GAS __attribute__((address_space(1)))
typedef float f32x16 __attribute__((ext_vector_type(16)));
typedef float f32x4 __attribute__((ext_vector_type(4)));
typedef float f32x2 __attribute__((ext_vector_type(2)));
typedef __bf16 bf16x8 __attribute__((ext_vector_type(8)));
typedef __bf16 bf16x2 __attribute__((ext_vector_type(2)));
typedef unsigned u32x4 __attribute__((ext_vector_type(4)));
typedef unsigned u32x2 __attribute__((ext_vector_type(2)));
typedef short s16x4 __attribute__((ext_vector_type(4)));
typedef unsigned long long u64;
typedef __amdgpu_buffer_rsrc_t rsrc_t;

constexpr int SEQ = 2048, DM = 448, NH = 16, DH = 28, FF = 1792;
constexpr unsigned WS_EPOCH = 0, WS_FQKV = 4096, WS_FH = WS_FQKV + 256 * 64, WS_FO = WS_FH + 256 * 64;
constexpr unsigned WS_KN2 = 512u << 10;
constexpr unsigned WS_Q = 1u << 20, WS_K = 3u << 20, WS_V = 5u << 20, WS_O = 7u << 20, WS_H = 9u << 20;
constexpr unsigned WS_BYTES = 17u << 20;
constexpr float C2 = 0.27264234f;
constexpr float LN_EPS = 1e-5f;
constexpr int AUX_SC1 = 16;
constexpr unsigned LDS_BYTES = 160 * 1024;
constexpr unsigned SPIN_MAX = 200000;
constexpr unsigned OOB = 0x7ffffff0u;
#define SAFE_BOUND 40.f
#define QD 1
#define QB 1
#define KQC 1
#define QX 1
#define QPF 3
#define XL 1
#define XB 1
#define XA 1

DI rsrc_t mk_rsrc(const void* p, unsigned bytes) { return __builtin_amdgcn_make_buffer_rsrc((void*)p, (short)0, (int)bytes, 0x00020000); }
DI unsigned rfl(unsigned x) { return __builtin_amdgcn_readfirstlane(x); }
DI unsigned rfl_pad(unsigned x) { unsigned r = rfl(x); asm volatile("s_nop 4" : "+s"(r)); return r; }
DI void dma16(rsrc_t rs, unsigned voff, unsigned soff, unsigned ldsaddr) {
    asm volatile("s_mov_b32 m0, %0\n\ts_nop 0\n\tbuffer_load_dwordx4 %1, %2, %3 offen lds" ::"s"(ldsaddr), "v"(voff), "s"(rs), "s"(soff) : "memory");
}
DI void dma16_sc1(rsrc_t rs, unsigned voff, unsigned soff, unsigned ldsaddr) {
    asm volatile("s_mov_b32 m0, %0\n\ts_nop 0\n\tbuffer_load_dwordx4 %1, %2, %3 offen sc1 lds" ::"s"(ldsaddr), "v"(voff), "s"(rs), "s"(soff) : "memory");
}
#define WAIT_VM0() asm volatile("s_waitcnt vmcnt(0)" ::: "memory")
#define BAR() asm volatile("s_waitcnt lgkmcnt(0)\n\ts_barrier" ::: "memory")
#define VM0_BAR() asm volatile("s_waitcnt vmcnt(0) lgkmcnt(0)\n\ts_barrier" ::: "memory")
#define VMCNT(n) asm volatile("s_waitcnt vmcnt(%0)" ::"n"(n) : "memory")
#define VMCNT_BAR(n) asm volatile("s_waitcnt vmcnt(%0) lgkmcnt(0)\n\ts_barrier" ::"n"(n) : "memory")
#define SBAR() __builtin_amdgcn_sched_barrier(0)
#define PIN(x) asm volatile("" : "+v"(x))

template <int V> using IC = std::integral_constant<int, V>;
template <int... Is, class F> DI void static_for_(std::integer_sequence<int, Is...>, F&& f) { (f(IC<Is>{}), ...); }
template <int N, class F> DI void static_for(F&& f) { static_for_(std::make_integer_sequence<int, N>{}, f); }

DI f32x16 mfma16(u32x4 a, u32x4 b, f32x16 c) { return __builtin_amdgcn_mfma_f32_32x32x16_bf16(__builtin_bit_cast(bf16x8, a), __builtin_bit_cast(bf16x8, b), c, 0, 0, 0); }
DI f32x16 mfma_f32(float a, float b, f32x16 c) { return __builtin_amdgcn_mfma_f32_32x32x2f32(a, b, c, 0, 0, 0); }
DI unsigned cvtpk(float lo, float hi) { f32x2 v = {lo, hi}; return __builtin_bit_cast(unsigned, __builtin_convertvector(v, bf16x2)); }
DI float dot2(unsigned a, unsigned b, float c) { return __builtin_amdgcn_fdot2_f32_bf16(__builtin_bit_cast(bf16x2, a), __builtin_bit_cast(bf16x2, b), c, false); }
DI float swap_add(float x) { auto r = __builtin_amdgcn_permlane32_swap(__float_as_uint(x), __float_as_uint(x), false, false); return __uint_as_float(r[0]) + __uint_as_float(r[1]); }
DI float swap_max(float x) { auto r = __builtin_amdgcn_permlane32_swap(__float_as_uint(x), __float_as_uint(x), false, false); return fmaxf(__uint_as_float(r[0]), __uint_as_float(r[1])); }
template <int CTRL> DI float dpp_max(float x) { return fmaxf(x, __uint_as_float(__builtin_amdgcn_update_dpp(0u, __float_as_uint(x), CTRL, 0xf, 0xf, true))); }
DI float max32(float x) {
    x = dpp_max<0x128>(x); x = dpp_max<0x124>(x); x = dpp_max<0x122>(x); x = dpp_max<0x121>(x);
    auto sw = __builtin_amdgcn_permlane16_swap(__float_as_uint(x), __float_as_uint(x), false, false);
    return fmaxf(__uint_as_float(sw[0]), __uint_as_float(sw[1]));
}
template <int D2> DI u32x4 tr_frag(LAS char* p) {
    s16x4 lo = __builtin_amdgcn_ds_read_tr16_b64_v4i16((LAS s16x4*)p), hi = __builtin_amdgcn_ds_read_tr16_b64_v4i16((LAS s16x4*)(p + D2));
    u32x2 a = __builtin_bit_cast(u32x2, lo), b = __builtin_bit_cast(u32x2, hi);
    return u32x4{a[0], a[1], b[0], b[1]};
}
DI u32x4 pack8(const f32x16& v, int s) { return u32x4{cvtpk(v[8 * s], v[8 * s + 1]), cvtpk(v[8 * s + 2], v[8 * s + 3]), cvtpk(v[8 * s + 4], v[8 * s + 5]), cvtpk(v[8 * s + 6], v[8 * s + 7])}; }
DI void st16(rsrc_t rs, unsigned off, u32x4 v) { __builtin_amdgcn_raw_buffer_store_b128(v, rs, off, 0, AUX_SC1); }
DI u32x4 ld16(rsrc_t rs, unsigned voff, unsigned soff) { return __builtin_amdgcn_raw_buffer_load_b128(rs, voff, soff, AUX_SC1); }
DI float bf2f(unsigned short b) { return __uint_as_float((unsigned)b << 16); }
DI u64 ld_flag(const char* p) { return __hip_atomic_load((GAS const u64*)p, __ATOMIC_RELAXED, __HIP_MEMORY_SCOPE_AGENT); }
DI void st_flag(char* p, u64 v) { __hip_atomic_store((GAS u64*)p, v, __ATOMIC_RELAXED, __HIP_MEMORY_SCOPE_AGENT); }
DI void poll_flags(const char* p, bool use, u64 tag) {
    for (unsigned n = 0; n < SPIN_MAX; ++n) {
        const u64 v = use ? ld_flag(p) : tag;
        if (__all(v == tag)) break;
        __builtin_amdgcn_s_sleep(1);
    }
    asm volatile("" ::: "memory");
}
template <class T> DI T* opaque(T* p) { asm volatile("" : "+v"(p)); return p; }
#define LDF(p) (*(LAS u32x4*)(p))

#define STAMP(i) do {} while (0)
#define FSTAMP(i) do {} while (0)
struct Ptrs { const void* x; const void* wqkv; const void* wo; const void* w1; const void* b1; const void* w2; const void* b2; void* out; char* ws; };

constexpr unsigned L_STAT = 155648, L_CS = 156672;
DI void sum8(const u32x4& f, float& a, float& b) {
    float t0, t1, t2, t3;
    asm volatile("v_lshlrev_b32 %2, 16, %6\n\tv_and_b32 %3, 0xffff0000, %6\n\tv_lshlrev_b32 %4, 16, %7\n\tv_and_b32 %5, 0xffff0000, %7\n\t"
                 "v_add_f32 %0, %0, %2\n\tv_add_f32 %1, %1, %3\n\tv_add_f32 %0, %0, %4\n\tv_add_f32 %1, %1, %5\n\t"
                 "v_lshlrev_b32 %2, 16, %8\n\tv_and_b32 %3, 0xffff0000, %8\n\tv_lshlrev_b32 %4, 16, %9\n\tv_and_b32 %5, 0xffff0000, %9\n\t"
                 "v_add_f32 %0, %0, %2\n\tv_add_f32 %1, %1, %3\n\tv_add_f32 %0, %0, %4\n\tv_add_f32 %1, %1, %5"
                 : "+v"(a), "+v"(b), "=&v"(t0), "=&v"(t1), "=&v"(t2), "=&v"(t3) : "v"(f[0]), "v"(f[1]), "v"(f[2]), "v"(f[3]));
}
DI void gelu_a(float a0, float a1, float rstd, float c0, float& x0, float& x1, float& e0, float& e1) {
    asm volatile("v_mul_f32 %0, %4, %6\n\tv_mul_f32 %1, %5, %6\n\tv_mul_f32 %2, %0, %0\n\tv_mul_f32 %3, %1, %1\n\t"
                 "v_fmamk_f32 %2, %2, 0x3dd2d3e8, %7\n\tv_fmamk_f32 %3, %3, 0x3dd2d3e8, %7\n\tv_mul_f32 %2, %0, %2\n\tv_mul_f32 %3, %1, %3\n\t"
                 "v_exp_f32 %2, -%2\n\tv_exp_f32 %3, -%3"
                 : "=&v"(x0), "=&v"(x1), "=&v"(e0), "=&v"(e1) : "v"(a0), "v"(a1), "v"(rstd), "v"(c0));
}
DI unsigned gelu_b(float x0, float x1, float e0, float e1) {
    unsigned r;
    asm volatile("v_add_f32 %1, 1.0, %1\n\tv_add_f32 %2, 1.0, %2\n\tv_rcp_f32 %1, %1\n\tv_rcp_f32 %2, %2\n\ts_nop 0\n\tv_mul_f32 %1, %3, %1\n\tv_mul_f32 %2, %4, %2\n\tv_cvt_pk_bf16_f32 %0, %1, %2"
                 : "=v"(r), "+v"(e0), "+v"(e1) : "v"(x0), "v"(x1));
    return r;
}
#define MFMA_F32_PAD(x) asm volatile("s_nop 15\n\ts_nop 3" : "+v"(x))
DI float fmul_s(float a, float b) { float r; asm("v_mul_f32 %0, %1, %2" : "=v"(r) : "v"(a), "v"(b)); return r; }
DI float ffma_s(float a, float b, float c) { float r; asm("v_fma_f32 %0, %1, %2, %3" : "=v"(r) : "v"(a), "v"(b), "v"(c)); return r; }
DI float fadd_s(float a, float b) { float r; asm("v_add_f32 %0, %1, %2" : "=v"(r) : "v"(a), "v"(b)); return r; }
struct P1Dma { rsrc_t rqkv, rw1, rw2, rwo; unsigned vq0, vq1, vq2, vf, qsoff, qdst, fsoff, fdst, tsoff, tdst, usoff, udst, tdummy, vt2, vto; };

struct QkvAddr { LAS char* qa[4]; LAS char* qa2[4]; LAS char* xu[4]; };
template <int HALF, bool SWA, bool SWB>
DI void p1_qkv_pass(const Ptrs& P, const P1Dma& D, u32x4 (&xf)[28], const QkvAddr& A, bool is28, f32x16& accA, f32x16& accB, float& csA, float& csB) {
    constexpr int PF = 3;
    u32x4 wa[PF], wb[PF];
    float a0 = 0.f, a1 = 0.f, b0 = 0.f, b1 = 0.f;
    const unsigned ones = 0x3F803F80u;
    accA = f32x16{}; accB = f32x16{};
    auto rd = [&](auto SC) {
        constexpr int s = decltype(SC)::value; constexpr int o = s < 12 ? (s & ~3) * 3072 : ((s & ~3) - 12) * 3072;
        wa[s % PF] = tr_frag<768>((s < 12 ? A.qa[s & 3] : A.qa2[s & 3]) + o); if (HALF == 0) wb[s % PF] = tr_frag<768>((s < 12 ? A.qa[s & 3] : A.qa2[s & 3]) + o + 56);
    };
    FSTAMP(16);
    rd(IC<0>{}); rd(IC<1>{}); rd(IC<2>{});
    SBAR();
    static_for<28>([&](auto SC) {
        constexpr int s = decltype(SC)::value;
        if constexpr (s == 20) FSTAMP(25);
        u32x4 ca = wa[s % PF]; const u32x4 cb = wb[s % PF];
        if (HALF == 1) { ca[0] = is28 ? ones : ca[0]; ca[1] = is28 ? ones : ca[1]; ca[2] = is28 ? ones : ca[2]; ca[3] = is28 ? ones : ca[3]; }
        accA = SWA ? mfma16(xf[s], ca, accA) : mfma16(ca, xf[s], accA);
        if constexpr (!QD) {} else if constexpr (s < 6) dma16(D.rqkv, s % 3 == 0 ? D.vq0 : s % 3 == 1 ? D.vq1 : D.vq2, D.qsoff + (s / 3) * (16 * 2688), D.qdst + s * 1024);
        else if constexpr (s >= 8 && s < 16) dma16(D.rw1, D.vf, D.fsoff + (s - 8) * (4 * 3584), D.fdst + (s - 8) * 1024);
        else if constexpr (s >= 16 && s < 20) dma16(D.rw1, D.vf, D.tsoff + (s - 16) * (4 * 3584), D.tdst + (s - 16) * 1024);
        if (HALF == 0) accB = SWB ? mfma16(xf[s], cb, accB) : mfma16(cb, xf[s], accB);
        else accB = mfma16(xf[s], xf[s], accB);
        if constexpr (s + PF < 28 && !(s + PF >= 12 && s <= 11)) rd(IC<(s + PF < 28 ? s + PF : 0)>{});
        if constexpr (QX && s >= 4 && s < 8) {
#pragma unroll
            for (int i = 0; i < 3; ++i) { constexpr int dummy = 0; (void)dummy; const int j = 3 * (s - 4) + i; xf[16 + j] = LDF(A.xu[j & 3] + 128 * (j >> 2)); }
        }
        if constexpr (KQC && (s & 3) == 0) { sum8(ca, a0, a1); if (HALF == 0) sum8(cb, b0, b1); }
        SBAR();
        if constexpr (s == 12) {
            asm volatile("s_mov_b32 m0, %0\n\ts_nop 0\n\tbuffer_load_dword %1, %2, 0 offen lds\n\tbuffer_load_dword %3, %4, 0 offen lds" ::"s"(D.tdummy), "v"(D.vt2), "s"(D.rw2), "v"(D.vto), "s"(D.rwo) : "memory");
        }
        if constexpr (QB && s == 3) { FSTAMP(17); VMCNT_BAR(4); FSTAMP(18); STAMP(18); SBAR(); }
        if constexpr (QB && s == 7) { FSTAMP(19); BAR(); FSTAMP(20); STAMP(19); SBAR(); }
        if constexpr (s == 11) {
            FSTAMP(21); if (QB) VMCNT_BAR(4); FSTAMP(24); STAMP(20);
            rd(IC<12>{}); rd(IC<13>{}); rd(IC<14>{});
            SBAR();
        }
    });
    csA = a0 + a1; csB = b0 + b1;
}
DI void p1_ffn_pass(const Ptrs& P, LAS char* lds, const P1Dma& D, const u32x4 (&xf)[28], unsigned tt, unsigned off, f32x16& accA, f32x16& accB, float& csA, float& csB,
                    char* flag, u64 tag) {
    LAS char* q[4]; LAS char* q2[4]; LAS char* p[4]; LAS char* p2[4];
#pragma unroll
    for (int u = 0; u < 4; ++u) {
        const unsigned ro = off + ((u + tt) & 3) * 4096;
        q[u] = opaque(lds + 49152 + ro); q2[u] = opaque(lds + 114688 + ro); p[u] = opaque(lds + 49152 + (ro ^ 64)); p2[u] = opaque(lds + 114688 + (ro ^ 64));
    }
    constexpr int PF = 3;
    u32x4 wa[PF], wb[PF];
    float a0 = 0.f, a1 = 0.f, b0 = 0.f, b1 = 0.f;
    accA = f32x16{}; accB = f32x16{};
    auto rd = [&](auto SC) {
        constexpr int s = decltype(SC)::value;
        if constexpr (s < 16) { wa[s % PF] = tr_frag<1024>(q[s & 3] + (s & ~3) * 4096); wb[s % PF] = tr_frag<1024>(p[s & 3] + (s & ~3) * 4096); }
        else if constexpr (s < 24) { wa[s % PF] = tr_frag<1024>(q2[s & 3] + ((s & ~3) - 16) * 4096); wb[s % PF] = tr_frag<1024>(p2[s & 3] + ((s & ~3) - 16) * 4096); }
        else { wa[s % PF] = tr_frag<1024>(q[s & 3] - 49152); wb[s % PF] = tr_frag<1024>(p[s & 3] - 49152); }
    };
    rd(IC<0>{}); rd(IC<1>{}); rd(IC<2>{});
    SBAR();
    static_for<28>([&](auto SC) {
        constexpr int s = decltype(SC)::value;
        const u32x4 ca = wa[s % PF], cb = wb[s % PF];
        accA = mfma16(ca, xf[s], accA);
        accB = mfma16(cb, xf[s], accB);
        if constexpr (s + PF < 28) rd(IC<(s + PF < 28 ? s + PF : 0)>{});
        if constexpr ((s & 3) == 0) sum8(ca, a0, a1);
        if constexpr ((s & 3) == 0) sum8(cb, b0, b1);
        SBAR();
        if constexpr (s == 12) {
            VMCNT_BAR(8);
            STAMP(21);
            SBAR();
        }
        if constexpr (s == 20) {
            VMCNT_BAR(0);
            if (threadIdx.x == 0) st_flag(flag, tag);
            SBAR();
        }
    });
    csA = a0 + a1; csB = b0 + b1;
}
DI float p1_cs(LAS float* csb, unsigned l31) {
    float v[8];
#pragma unroll
    for (int i = 0; i < 8; ++i) v[i] = csb[i * 32 + l31];
    return ((v[0] + v[1]) + (v[2] + v[3])) + ((v[4] + v[5]) + (v[6] + v[7]));
}
struct P1Ctx { rsrc_t rws; char* ws; unsigned lane, l31, h, tile; float mu, sig, rstd; };
DI void p1_gelu(const P1Ctx& C, f32x16 acc, float cs, float bias, u32x4& pk0, u32x4& pk1) {
    acc = mfma_f32(C.h ? bias : cs, C.h ? C.sig : -C.mu, acc);
#pragma unroll
    for (int i = 0; i < 16; i += 2) {
        const f32x2 xv = f32x2{acc[i], acc[i + 1]} * C.rstd, t2 = xv * xv;
        const f32x2 pl = __builtin_elementwise_fma(t2, f32x2{0.1029432397f, 0.1029432397f}, f32x2{2.302208198f, 2.302208198f}), z = xv * pl;
        const f32x2 d = f32x2{__builtin_amdgcn_exp2f(-z[0]), __builtin_amdgcn_exp2f(-z[1])} + 1.f;
        const f32x2 y = xv * f32x2{__builtin_amdgcn_rcpf(d[0]), __builtin_amdgcn_rcpf(d[1])};
        acc[i] = y[0]; acc[i + 1] = y[1];
    }
    pk0 = pack8(acc, 0); pk1 = pack8(acc, 1);
}
DI void p1_store_h(const P1Ctx& C, unsigned ft, u32x4 pk0, u32x4 pk1) {
    const unsigned off = WS_H + (C.tile * 112 + 2 * ft) * 1024 + C.lane * 16;
    st16(C.rws, off, pk0); st16(C.rws, off + 1024, pk1);
}
DI void p1_epi_qkv(const P1Ctx& C, f32x16 acc, float cs, unsigned type, unsigned head) {
    const unsigned h = C.h, l31 = C.l31;
    const bool isv = type == 2;
    const float m0 = h ? 0.f : -C.mu, c0 = h ? 0.f : cs, r0 = h ? 0.f : (type == 0 ? C.rstd * C2 : C.rstd), k0 = (h || l31 >= 28) ? 0.f : 1.f;
    acc = mfma_f32(isv ? m0 : c0, isv ? c0 : m0, acc);
    f32x16 R = {}; R = mfma_f32(isv ? r0 : k0, isv ? k0 : r0, R);
    const float pv = (isv && l31 == 28) ? 1.f : 0.f, p12 = isv ? pv : ((type == 1 && h) ? 1.f : 0.f);
    f32x2 n2 = {0.f, 0.f};
#pragma unroll
    for (int i = 0; i < 16; i += 2) {
        const f32x2 t = __builtin_elementwise_fma(f32x2{acc[i], acc[i + 1]}, f32x2{R[i], R[i + 1]}, f32x2{i == 12 ? p12 : pv, pv});
        acc[i] = t[0]; acc[i + 1] = t[1];
        n2 = __builtin_elementwise_fma(t, t, n2);
    }
    const unsigned off = WS_Q + type * (2u << 20) + (head * 64 + C.tile) * 2048 + C.lane * 16;
    st16(C.rws, off, pack8(acc, 0)); st16(C.rws, off + 1024, pack8(acc, 1));
    const float kn = max32(swap_add(n2[0] + n2[1]));
    __builtin_amdgcn_raw_buffer_store_b32(__float_as_uint(kn), C.rws, (type == 1 && C.lane == 0) ? WS_KN2 + (head * 64 + C.tile) * 4 : OOB, 0, AUX_SC1);
}

DI u64 make_tag(u64 e) {
    e = ((u64)rfl((unsigned)(e >> 32)) << 32) | rfl((unsigned)e);
    u64 tag = (e + 0x9E3779B97F4A7C15ull); tag = (tag ^ (tag >> 30)) * 0xBF58476D1CE4E5B9ull; tag = (tag ^ (tag >> 27)) * 0x94D049BB133111EBull; tag ^= tag >> 31;
    return tag;
}
constexpr unsigned p3_region(int sb) { return (sb % 3) * 49152u; }
constexpr unsigned RING = 98304;
constexpr unsigned ring_off(int c) { return c % 4 == 2 ? 0u : c % 4 == 0 ? 16384u : c % 4 == 1 ? 32768u : 49152u; }
struct P3W { rsrc_t rw2, rwo; unsigned vw, vo0, vo1, soff, dst, hd0; };
template <int SB, int I> DI void p3_dma(const P3W& W) {
    constexpr int j = 6 * SB + I;
    if constexpr (j < 28) dma16(W.rw2, W.vw, W.soff + j * (16 * DM * 2), W.dst + p3_region(SB) + I * 2048);
    else {
        dma16(W.rwo, ((j - 28) & 1) ? W.vo1 : W.vo0, W.hd0 + ((j - 28) >> 1) * (28 * DM * 2), W.dst + p3_region(SB) + I * 2048);
    }
}
DI P3W make_p3w(const Ptrs& P, unsigned lds0, int b, unsigned w, unsigned lane) {
    const unsigned r = b >> 3, cg3 = r < 28 ? r >> 2 : 0;
    P3W W3;
    const unsigned vcol = cg3 * 128 + (((lane >> 2) & 1) ^ ((lane >> 4) & 1)) * 64 + (lane & 3) * 16, rw = (w & 1) * 8 + (lane >> 3);
    W3.rw2 = mk_rsrc(P.w2, FF * DM * 2); W3.rwo = mk_rsrc(P.wo, DM * DM * 2);
    W3.vw = rw * (DM * 2) + vcol; W3.vo0 = W3.vw; W3.vo1 = W3.vw + 16 * (DM * 2);
    W3.soff = rfl_pad(28 * (w >> 1) * (16 * DM * 2)); W3.hd0 = rfl_pad(4 * (w >> 1) * (28 * DM * 2));
    W3.dst = rfl_pad(lds0 + (w >> 1) * 6 * 2048 + (w & 1) * 1024);
    return W3;
}
DI void kv_first(const Ptrs& P, LAS char* lds, int b, unsigned w, unsigned lane, u32x4& qf0, u32x4& qf1, float& kn2) {
    const unsigned g = b & 7, r = b >> 3, head = 2 * g + (r >> 4), tile = (r & 15) * 4 + (w & 3);
    const rsrc_t rws = mk_rsrc(P.ws, WS_BYTES);
    const unsigned src = rfl_pad(((w & 1) ? WS_V : WS_K) + (head * 64 + (w >> 2) * 32 + ((w >> 1) & 1)) * 2048), wdst = rfl_pad((unsigned)(size_t)lds + 98304 + w * 2048);
    dma16_sc1(rws, lane * 16, src, wdst + 16384); dma16_sc1(rws, lane * 16 + 1024, src, wdst + 16384 + 1024);
    dma16_sc1(rws, lane * 16, src + 4096, wdst + 32768); dma16_sc1(rws, lane * 16 + 1024, src + 4096, wdst + 32768 + 1024);
    const unsigned qs = rfl_pad(WS_Q + (head * 64 + tile) * 2048), ks = rfl_pad(WS_KN2 + head * 256);
    asm volatile("buffer_load_dwordx4 %0, %3, %5, %6 offen sc1\n\tbuffer_load_dwordx4 %1, %3, %5, %6 offen offset:1024 sc1\n\tbuffer_load_dword %2, %4, %5, %7 offen sc1"
                 : "=&v"(qf0), "=&v"(qf1), "=&v"(kn2) : "v"(lane * 16), "v"(lane * 4), "s"(rws), "s"(qs), "s"(ks) : "memory");
}
DI u64 phase1(const Ptrs& P, LAS char* lds, int b, u32x4& qf0, u32x4& qf1, float& kn2, P3W& W3) {
    const unsigned tid = threadIdx.x, lane = tid & 63, l31 = lane & 31, h = lane >> 5;
    const unsigned w = rfl(tid >> 6), tt = w & 3, half = w >> 2;
    const unsigned g = b & 7, r = b >> 3, tb = 4 * (g >> 1) + (r & 3), cg = 8 * (g & 1) + (r >> 2);
    const unsigned lds0 = (unsigned)(size_t)lds;
    const rsrc_t rx = mk_rsrc(P.x, SEQ * DM * 2);
    P1Dma D;
    D.rqkv = mk_rsrc(P.wqkv, DM * 3 * DM * 2);
    const unsigned cb = 168 * cg, cb16 = cb & ~15u, dl = cb & 15u;
    FSTAMP(13);
    {
        const unsigned rowa = 2 * w + h;
        const unsigned va = rowa * 896 + ((l31 ^ (rowa & 15)) << 4), sa = tb * 128 * 896;
#pragma unroll
        for (unsigned i = 0; i < 8; ++i) dma16(rx, va, sa + i * (16 * 896), lds0 + (w + 8 * i) * 1024);
    }
    {
        auto vq = [&](unsigned p) { const unsigned row = p / 12, ch = p - row * 12; return ch < 11 ? row * 2688 + cb16 + ch * 16 : OOB; };
        D.vq0 = vq(lane); D.vq1 = vq(64 + lane); D.vq2 = vq(128 + lane);
    }
    {
        const unsigned so = w * (16 * 2688), ds = lds0 + 114688 + w * 3072;
        dma16(D.rqkv, D.vq0, so, ds); dma16(D.rqkv, D.vq1, so, ds + 1024); dma16(D.rqkv, D.vq2, so, ds + 2048);
        if (w < 4) { dma16(D.rqkv, D.vq0, so + 8 * (16 * 2688), ds + 8 * 3072); dma16(D.rqkv, D.vq1, so + 8 * (16 * 2688), ds + 8 * 3072 + 1024); dma16(D.rqkv, D.vq2, so + 8 * (16 * 2688), ds + 8 * 3072 + 2048); }
    }
    const unsigned ntile = ((cg ^ (cg >> 1)) & 1) ? 3 : 4, fstart = 7 * (cg >> 1) + ((cg & 1) ? (((cg >> 1) & 1) ? 3 : 4) : 0);
    D.rw1 = mk_rsrc(P.w1, DM * FF * 2);
    { const unsigned rq = lane >> 4, sl = ((lane >> 2) & 3) ^ rq; D.vf = sl < ntile ? rq * 3584 + (fstart + sl) * 64 + (lane & 3) * 16 : OOB; }
    D.qsoff = (12 + 2 * w) * (16 * 2688); D.qdst = lds0 + 2 * w * 3072;
    D.fsoff = 2 * w * (16 * 3584); D.fdst = lds0 + 49152 + 2 * w * 4096;
    D.tsoff = (256 + 16 * w) * 3584; D.tdst = lds0 + 114688 + w * 4096;
    D.usoff = (384 + 8 * w) * 3584; D.udst = lds0 + 2 * w * 1024;
    D.rw2 = mk_rsrc(P.w2, FF * DM * 2); D.rwo = mk_rsrc(P.wo, DM * DM * 2); D.tdummy = lds0 + 151552 + w * 256;
    { const unsigned i = w * 8 + (lane & 7); D.vt2 = (lane < 8 && i < 49) ? (b * 49 + i) * 128 : OOB; D.vto = (lane < 8 && i < 13) ? (b * 13 + i) * 128 : OOB; }
    const u64 eraw = ld_flag(P.ws + WS_EPOCH);
    const unsigned short b1a = ((const unsigned short*)P.b1)[(fstart + 2 * half) * 32 + l31];
    const unsigned short b1b = ((const unsigned short*)P.b1)[(2 * half + 1 < ntile ? fstart + 2 * half + 1 : fstart) * 32 + l31];
#pragma unroll
    for (unsigned i = 0; i < 6; ++i) {
        const unsigned p = 64 * i + lane, rr = p / 24, c = p - rr * 24, row = 16 * w + rr;
        dma16(rx, row * 896 + 512 + ((c ^ ((row >> 1) & 7)) << 4), tb * 128 * 896, lds0 + 65536 + (6 * w + i) * 1024);
    }
    const rsrc_t rws = mk_rsrc(P.ws, WS_BYTES);
    LAS float* stat = (LAS float*)(lds + L_STAT);
    LAS float* csb = (LAS float*)(lds + L_CS);
    P1Ctx C{rws, P.ws, lane, l31, h, tb * 4 + tt, 0.f, 0.f, 0.f};
    const unsigned G = lane >> 4, q4 = (lane & 15) >> 2;
    const unsigned trq = (8 * (G >> 1) + q4) * 192 + dl + (G & 1) * 32 + (lane & 3) * 8 + half * 112;
    const unsigned trf = (8 * (G >> 1) + q4) * 256 + (G & 1) * 32 + (lane & 3) * 8;
    f32x16 accA, accB; float csA, csB;
    const unsigned tA = 3 * cg + 2 * half, tB = 3 * cg + 1, tyA = tA >> 4, tyB = tB >> 4;
    QkvAddr QA;
    {
        LAS char* xb0 = lds + 65536 + (tt * 32 + l31) * 384; const unsigned gswb = (l31 >> 1) & 7;
#pragma unroll
        for (int u = 0; u < 4; ++u) {
            const unsigned ro = ((u + tt) & 3) * 3072;
            QA.qa[u] = opaque(lds + 114688 + trq + ro); QA.qa2[u] = opaque(lds + trq + ro);
            QA.xu[u] = opaque(xb0 + (((2 * ((u + tt) & 3) + h) ^ gswb) << 4));
        }
    }
    const bool m28 = l31 == 28;
    FSTAMP(14);
    VMCNT_BAR(6);
    STAMP(1);
    u32x4 xf[28];
    {
        LAS char* xa = lds + (tt * 32 + l31) * 512;
#pragma unroll
        for (int u = 0; u < 4; ++u) {
            const unsigned c = 2 * ((u + tt) & 3) + h;
            LAS char* x0 = xa + ((c ^ (l31 & 15)) << 4); LAS char* x1 = xa + (((c + 8) ^ (l31 & 15)) << 4);
            xf[u] = LDF(x0); xf[4 + u] = LDF(x1); xf[8 + u] = LDF(x0 + 256); xf[12 + u] = LDF(x1 + 256);
        }
    }
    const u64 tag = make_tag(eraw);
    BAR();
    STAMP(2);
    if (half == 0) {
        if (tyA == 2) p1_qkv_pass<0, true, true>(P, D, xf, QA, m28, accA, accB, csA, csB);
        else if (tyB == 2) p1_qkv_pass<0, false, true>(P, D, xf, QA, m28, accA, accB, csA, csB);
        else p1_qkv_pass<0, false, false>(P, D, xf, QA, m28, accA, accB, csA, csB);
        csb[0 * 256 + tt * 64 + lane] = csA; csb[1 * 256 + tt * 64 + lane] = csB;
    } else {
        float a1;
        if (tyA == 2) {
            p1_qkv_pass<1, true, false>(P, D, xf, QA, m28, accA, accB, csA, csB);
            if (l31 == 28) {
#pragma unroll
                for (int i = 0; i < 16; ++i) stat[(tt * 32 + (i & 3) + 8 * (i >> 2) + 4 * h) * 2] = accA[i];
            }
            asm volatile("s_waitcnt lgkmcnt(0)" ::: "memory");
            a1 = stat[(tt * 32 + l31) * 2];
        } else {
            p1_qkv_pass<1, false, false>(P, D, xf, QA, m28, accA, accB, csA, csB);
            auto sw = __builtin_amdgcn_permlane32_swap(__float_as_uint(accA[12]), __float_as_uint(accA[12]), false, false);
            a1 = __uint_as_float(sw[1]);
        }
        csb[2 * 256 + tt * 64 + lane] = csA;
        const unsigned ri = (l31 & 3) + 4 * (l31 >> 3);
        float dg = 0.f;
#pragma unroll
        for (int i = 0; i < 16; ++i) dg = ri == (unsigned)i ? accB[i] : dg;
        const float a2 = swap_add(h == ((l31 >> 2) & 1) ? dg : 0.f);
        const float m = a1 * (1.f / DM), var = fmaxf(a2 * (1.f / DM) - m * m, 0.f) + LN_EPS;
        if (!h) *(LAS f32x2*)(stat + (tt * 32 + l31) * 2) = f32x2{m, __builtin_amdgcn_rsqf(var)};
    }
    FSTAMP(2);
    VMCNT_BAR(4);
    STAMP(3);
    dma16(D.rw1, D.vf, D.usoff, D.udst); dma16(D.rw1, D.vf, D.usoff + 4 * 3584, D.udst + 1024);
    { const f32x2 st = *(LAS f32x2*)(stat + (tt * 32 + l31) * 2); C.mu = st[0]; C.rstd = st[1]; C.sig = __builtin_amdgcn_rcpf(st[1]); }
    {
        const float c0 = p1_cs(csb + 2 * half * 256, l31), c1 = p1_cs(csb + 256, l31);
        p1_epi_qkv(C, accA, c0, tyA, tA & 15);
        if (half == 0) p1_epi_qkv(C, accB, c1, tyB, tB & 15);
        else asm volatile("buffer_store_dword %0, %0, %1, 0 offen sc1\n\tbuffer_store_dword %0, %0, %1, 0 offen sc1\n\tbuffer_store_dword %0, %0, %1, 0 offen sc1" ::"v"(OOB), "s"(rws) : "memory");
    }
    FSTAMP(5);
    STAMP(4);
    const unsigned head2 = 2 * g + (r >> 4), qb2 = r & 15;
    const char* qflag;
    { const unsigned cq = head2 / 3, ck = (16 + head2) / 3, cv = (32 + head2) / 3;
      const unsigned idx = lane < 16 ? ck * 16 + lane : lane < 32 ? cv * 16 + (lane - 16) : cq * 16 + qb2; qflag = P.ws + WS_FQKV + (lane <= 32 ? idx : 0) * 64; }
    p1_ffn_pass(P, lds, D, xf, tt, trf + 64 * ((2 * half) ^ q4), accA, accB, csA, csB, P.ws + WS_FQKV + (cg * 16 + tb) * 64, tag);
    csb[(3 + 2 * half) * 256 + tt * 64 + lane] = csA; csb[(4 + 2 * half) * 256 + tt * 64 + lane] = csB;
    W3 = make_p3w(P, lds0, b, w, lane);
    STAMP(22);
    BAR();
    STAMP(23);
    {
        const bool second = 2 * half + 1 < ntile;
        const float c0 = p1_cs(csb + (3 + 2 * half) * 256, l31), c1 = p1_cs(csb + (4 + 2 * half) * 256, l31);
        u32x4 pk0, pk1;
        const u64 fv = ld_flag(qflag);
        p1_gelu(C, accA, c0, bf2f(b1a), pk0, pk1);
        PIN(pk0); PIN(pk1);
        FSTAMP(27);
        if (!__all(lane > 32 || fv == tag)) poll_flags(qflag, lane <= 32, tag);
        FSTAMP(28);
        kv_first(P, lds, b, w, lane, qf0, qf1, kn2);
        FSTAMP(29);
        p1_store_h(C, fstart + 2 * half, pk0, pk1);
        if (second) { p1_gelu(C, accB, c1, bf2f(b1b), pk0, pk1); p1_store_h(C, fstart + 2 * half + 1, pk0, pk1); }
        else { asm volatile("buffer_store_dword %0, %0, %1, 0 offen sc1\n\tbuffer_store_dword %0, %0, %1, 0 offen sc1" ::"v"(OOB), "s"(rws) : "memory"); }
    }
    FSTAMP(12);
    STAMP(6);
    return tag;
}

constexpr int att_aflo(int T) { return (!XA || T < 6) ? 0 : (T - 6) * 28 / 26; }
constexpr int att_kv(int T) { return (XL && (T & 1) == 0 && (T >> 1) + 3 < 16) ? 2 : 0; }
constexpr int att_w(int T) { return T < 12 ? 1 : 0; }
constexpr int att_all(int T0, int T1) { int n = 0; for (int T = T0; T < T1; ++T) n += att_kv(T) + att_w(T) + (att_aflo(T + 1) - att_aflo(T)); return n; }
#define EXP4(S, i) do { S[i] = __builtin_amdgcn_exp2f(S[i]); S[i + 1] = __builtin_amdgcn_exp2f(S[i + 1]); S[i + 2] = __builtin_amdgcn_exp2f(S[i + 2]); S[i + 3] = __builtin_amdgcn_exp2f(S[i + 3]); } while (0)
template <int KH>
DI void att_fast(LAS char* lds, rsrc_t rws, const P3W& W3, unsigned lane, unsigned w, unsigned src, unsigned wdst, unsigned afsrc, u32x4 qf0, u32x4 qf1, f32x16& o, u32x4 (&af)[28], const char* hflag, char* myh, u64 tag) {
    LAS char* kvb = opaque(lds + RING + KH * 8192 + lane * 16);
    const unsigned vo = lane * 16, vo1 = lane * 16 + 1024;
    src = rfl_pad(src); wdst = rfl_pad(wdst); afsrc = rfl_pad(afsrc);
    const f32x16 zero = {};
    f32x16 sc[2];
    u32x4 k0 = LDF(kvb + ring_off(0)), k1 = LDF(kvb + ring_off(0) + 1024), v0 = LDF(kvb + ring_off(0) + 2048), v1 = LDF(kvb + ring_off(0) + 3072), p1 = {0u, 0u, 0u, 0u}, v1p = {0u, 0u, 0u, 0u};
    sc[0] = mfma16(k0, qf0, zero); sc[0] = mfma16(k1, qf1, sc[0]);
    k0 = LDF(kvb + ring_off(0) + 4096); k1 = LDF(kvb + ring_off(0) + 4096 + 1024);
    static_for<32>([&](auto TC) {
        constexpr int T = decltype(TC)::value, c = T >> 1;
        f32x16& S = sc[T & 1]; f32x16& SN = sc[(T + 1) & 1];
        constexpr int o1 = ring_off((T + 1) >> 1) + ((T + 1) & 1) * 4096, o2 = ring_off((T + 2) >> 1) + ((T + 2) & 1) * 4096;
        u32x4 p0, k0n, k1n, v0n, v1n;
        auto chunk_barrier = [&](auto OPS) {
            constexpr int younger = decltype(OPS)::value;
            if constexpr (T == 5 && KH == 0) { if (w == 0) { PIN(p1); PIN(o); poll_flags(hflag, lane < 16, tag); } }
            if (XB) asm volatile("s_waitcnt vmcnt(%0) lgkmcnt(%1)\n\ts_barrier" ::"n"(younger), "n"(KH == 0 ? 4 : 2) : "memory");
            if constexpr (T == 1 && KH == 0) { if (w == 0) { PIN(p1); PIN(o); if (lane == 0) st_flag(myh, tag); } }
        };
        S[0] = __builtin_amdgcn_exp2f(S[0]); S[1] = __builtin_amdgcn_exp2f(S[1]);
        if (T > 0) o = mfma16(v1p, p1, o);
        S[2] = __builtin_amdgcn_exp2f(S[2]); S[3] = __builtin_amdgcn_exp2f(S[3]);
        if (T + 2 < 32) { k0n = LDF(kvb + o2); k1n = LDF(kvb + o2 + 1024); }
        p0[0] = cvtpk(S[0], S[1]); p0[1] = cvtpk(S[2], S[3]); SBAR();
        if (T + 1 < 32) SN = mfma16(k0, qf0, zero);
        EXP4(S, 4);
        if constexpr (att_kv(T) != 0) { dma16_sc1(rws, vo, src + (c + 3) * 4096, wdst + ring_off(c + 3)); dma16_sc1(rws, vo1, src + (c + 3) * 4096, wdst + ring_off(c + 3) + 1024); }
        if constexpr (T < 12) p3_dma<T / 6, T % 6>(W3);
        static_for<att_aflo(T + 1) - att_aflo(T)>([&](auto JC) { constexpr int j = att_aflo(T) + decltype(JC)::value; af[j] = ld16(rws, vo + (j & 3) * 1024, afsrc + (j >> 2) * 4096); });
        p0[2] = cvtpk(S[4], S[5]); p0[3] = cvtpk(S[6], S[7]); SBAR();
        constexpr int younger = c >= 1 ? att_all(2 * c - 2, T + 1) - att_kv(2 * c - 2) : att_all(0, T + 1);
        if constexpr (KH == 1 && (T & 1) == 1 && T < 31) chunk_barrier(IC<younger>{});
        if (T + 1 < 32) SN = mfma16(k1, qf1, SN);
        EXP4(S, 8);
        if (T + 1 < 32) { v0n = LDF(kvb + o1 + 2048); v1n = LDF(kvb + o1 + 3072); }
        p1[0] = cvtpk(S[8], S[9]); p1[1] = cvtpk(S[10], S[11]); SBAR();
        o = mfma16(v0, p0, o);
        EXP4(S, 12);
        p1[2] = cvtpk(S[12], S[13]); p1[3] = cvtpk(S[14], S[15]); SBAR();
        v1p = v1;
        if (T + 2 < 32) { k0 = k0n; k1 = k1n; }
        if (T + 1 < 32) { v0 = v0n; v1 = v1n; }
        if constexpr (KH == 0 && (T & 1) == 1 && T < 31) chunk_barrier(IC<younger>{});
    });
    o = mfma16(v1p, p1, o);
}

DI void phase23(const Ptrs& P, LAS char* lds, int b, u64 tag, u32x4 qf0, u32x4 qf1, float kn2, const P3W& W3) {
    const unsigned tid = threadIdx.x, lane = tid & 63, l31 = lane & 31, h = lane >> 5;
    const unsigned w = rfl(tid >> 6), qt = w & 3, kh = w >> 2;
    const unsigned g = b & 7, r = b >> 3, head = 2 * g + (r >> 4), qb = r & 15, tile = qb * 4 + qt;
    const unsigned lds0 = (unsigned)(size_t)lds;
    const rsrc_t rws = mk_rsrc(P.ws, WS_BYTES);
    const bool p3 = r < 28;
    const unsigned tt3 = w & 1, q3 = w >> 1, tb3 = 4 * g + (r & 3), cg3 = p3 ? r >> 2 : 0, tile3 = tb3 * 2 + tt3;
    STAMP(7);
    const unsigned src = rfl_pad(((w & 1) ? WS_V : WS_K) + (head * 64 + kh * 32 + ((w >> 1) & 1)) * 2048);
    const unsigned wdst = rfl_pad(lds0 + RING + w * 2048);
#define STAGE(c, off) do { dma16_sc1(rws, lane * 16, src + (c) * 4096, wdst + (off)); dma16_sc1(rws, lane * 16 + 1024, src + (c) * 4096, wdst + (off) + 1024); } while (0)
    asm volatile("s_waitcnt vmcnt(4)" : "+v"(qf0), "+v"(qf1), "+v"(kn2)::"memory");
    kn2 = swap_max(max32(kn2));
    float qn2 = 0.f;
#pragma unroll
    for (int e = 0; e < 4; ++e) { qn2 = dot2(qf0[e], qf0[e], qn2); qn2 = dot2(qf1[e], qf1[e], qn2); }
    qn2 = swap_add(qn2);
    const float bound = __builtin_amdgcn_sqrtf(qn2 * kn2) * 1.01f + 0.01f;
    const bool safe = __all(bound <= SAFE_BOUND);
    float mrun;
    if (safe) {
        const unsigned nb = cvtpk(-bound, 0.f) & 0xffffu;
        if (h) qf1[2] = (qf1[2] & 0xffff0000u) | nb;
        mrun = __uint_as_float((nb & 0x7fffu) << 16);
    } else mrun = -INFINITY;
    PIN(qf1); PIN(mrun);
    STAGE(2, ring_off(2));
    BAR();
    char* myh = P.ws + WS_FH + ((4 * (g >> 1) + (r & 3)) * 16 + 8 * (g & 1) + (r >> 2)) * 64;
    STAMP(8);
    f32x16 o = {};
    u32x4 af[28];
    const unsigned afsrc = WS_H + (tile3 * 112 + 28 * q3) * 1024;
    const char* hflag = P.ws + WS_FH + ((tb3 >> 1) * 16 + (lane & 15)) * 64;
    if (safe) {
        if (kh == 0) att_fast<0>(lds, rws, W3, lane, w, src, wdst, afsrc, qf0, qf1, o, af, hflag, myh, tag);
        else att_fast<1>(lds, rws, W3, lane, w, src, wdst, afsrc, qf0, qf1, o, af, hflag, myh, tag);
    } else {
        const f32x16 zero = {};
        LAS char* kvb = lds + RING + kh * 8192 + lane * 16;
        static_for<12>([&](auto TC) { constexpr int T = decltype(TC)::value; p3_dma<T / 6, T % 6>(W3); });
#pragma unroll 1
        for (unsigned c = 0; c < 16; ++c) {
            auto roff = [](unsigned c) { return (c & 3) == 2 ? 0u : (c & 3) == 0 ? 16384u : (c & 3) == 1 ? 32768u : 49152u; };
            if (c + 3 < 16) STAGE(c + 3, roff(c + 3));
#pragma unroll 1
            for (int t = 0; t < 2; ++t) {
                LAS char* kv = kvb + roff(c) + t * 4096;
                const u32x4 k0 = LDF(kv), k1 = LDF(kv + 1024), v0 = LDF(kv + 2048), v1 = LDF(kv + 3072);
                f32x16 s = mfma16(k0, qf0, zero); s = mfma16(k1, qf1, s);
                float m = s[0];
#pragma unroll
                for (int i = 1; i < 16; ++i) m = fmaxf(m, s[i]);
                m = fmaxf(swap_max(m), mrun);
                const float f = __builtin_amdgcn_exp2f(mrun - m);
                mrun = m;
#pragma unroll
                for (int i = 0; i < 16; ++i) { s[i] = __builtin_amdgcn_exp2f(s[i] - m); o[i] *= f; }
                o = mfma16(v0, pack8(s, 0), o); o = mfma16(v1, pack8(s, 1), o);
            }
            if (c == 2 && w == 0) poll_flags(hflag, lane < 16, tag);
            if (c < 15) VM0_BAR();
            if (c == 0 && tid == 0) st_flag(myh, tag);
        }
#pragma unroll
        for (int j = 0; j < 28; ++j) af[j] = ld16(rws, lane * 16, afsrc + j * 1024);
    }
    BAR();
    STAMP(9);
    LAS float* ex = (LAS float*)(lds + 147456 + qt * 4096);
    if (kh == 1) {
        if (h) o[15] = mrun;
#pragma unroll
        for (int i = 0; i < 16; ++i) ex[i * 64 + lane] = o[i];
    }
    BAR();
    if (kh == 0) {
        const float m2 = ex[15 * 64 + 32 + l31], m = fmaxf(mrun, m2), f1 = __builtin_amdgcn_exp2f(mrun - m), f2 = __builtin_amdgcn_exp2f(m2 - m);
#pragma unroll
        for (int i = 0; i < 16; ++i) o[i] = o[i] * f1 + ex[i * 64 + lane] * f2;
        auto sw = __builtin_amdgcn_permlane32_swap(__float_as_uint(o[12]), __float_as_uint(o[12]), false, false);
        const float l = __uint_as_float(sw[1]);
        const float il = __builtin_amdgcn_rcpf(l);
#pragma unroll
        for (int i = 0; i < 16; ++i) o[i] *= il;
        if (h) { o[12] = 0.f; o[13] = 0.f; o[14] = 0.f; o[15] = 0.f; }
        const unsigned off = WS_O + (tile * 16 + head) * 2048 + lane * 16;
        st16(rws, off, pack8(o, 0)); st16(rws, off + 1024, pack8(o, 1));
    }
    STAMP(10);
    if (!p3) {
        VM0_BAR();
        if (tid == 0) st_flag(P.ws + WS_FO + (qb * 16 + head) * 64, tag);
        if (b == 255 && w == 0) {
#pragma unroll 1
            for (int i = 0; i < 4; ++i) poll_flags(P.ws + WS_FQKV + (i * 64 + lane) * 64, true, tag);
            if (lane == 0) st_flag(P.ws + WS_EPOCH, tag);
        }
        return;
    }
    const unsigned q4 = (lane & 15) >> 2, trb = (4 * (lane >> 5) + q4) * 128 + ((lane >> 4) & 1) * 32 + (lane & 3) * 8 + q3 * 12 * 1024;
    const unsigned tro0 = trb + ((q4 >> 1) & 1) * 64, tro1 = trb + (((q4 >> 1) & 1) ^ 1) * 64;
    const unsigned tok = tile3 * 32 + l31, n0 = cg3 * 64 + q3 * 16;
    u32x2 xr[2], br[2];
#pragma unroll
    for (int gq = 0; gq < 2; ++gq) {
        const unsigned n = n0 + 8 * gq + 4 * h;
        xr[gq] = *(const u32x2*)((const char*)P.x + (tok * DM + n) * 2); br[gq] = *(const u32x2*)((const char*)P.b2 + n * 2);
    }
    f32x16 acc0 = {}, acc1 = {};
    u32x4 ao[8];
    static_for<6>([&](auto SC) {
        constexpr int sb = decltype(SC)::value;
        LAS char* wp0 = opaque(lds + p3_region(sb) + tro0); LAS char* wp1 = opaque(lds + p3_region(sb) + tro1);
        u32x4 w0[3], w1[3];
#pragma unroll
        for (int i = 0; i < 3; ++i) { w0[i] = tr_frag<1024>(wp0 + i * 2048); w1[i] = tr_frag<1024>(wp1 + i * 2048); }
        if (sb == 3) {
#pragma unroll
            for (int j = 0; j < 8; ++j) ao[j] = ld16(rws, lane * 16 + (j & 3) * 1024, WS_O + (tile3 * 32 + 8 * q3) * 1024 + (j >> 2) * 4096);
        }
        SBAR();
        static_for<6>([&](auto IC_) {
            constexpr int i = decltype(IC_)::value, j = 6 * sb + i;
            const u32x4 a = j < 28 ? af[j < 28 ? j : 0] : ao[j >= 28 ? j - 28 : 0];
            acc0 = mfma16(w0[i % 3], a, acc0);
            if constexpr (sb + 2 < 6) p3_dma<(sb + 2 < 6 ? sb + 2 : 0), i>(W3);
            acc1 = mfma16(w1[i % 3], a, acc1);
            if (i + 3 < 6) { w0[i % 3] = tr_frag<1024>(wp0 + (i + 3) * 2048); w1[i % 3] = tr_frag<1024>(wp1 + (i + 3) * 2048); }
            SBAR();
        });
        if (sb == 2 && w == 0) poll_flags(P.ws + WS_FO + ((tb3 >> 1) * 16 + (lane & 15)) * 64, lane < 16, tag);
        if (sb < 4) asm volatile("s_waitcnt vmcnt(6) lgkmcnt(0)\n\ts_barrier" ::: "memory");
        else if (sb == 4) VM0_BAR();
        if (sb == 0 && tid == 0) st_flag(P.ws + WS_FO + (qb * 16 + head) * 64, tag);
        if (sb == 2) STAMP(11);
        if (sb == 4) STAMP(12);
    });
    BAR();
    STAMP(13);
    LAS float* rx = (LAS float*)lds;
    float v[8];
    auto scatter = [&](auto QC_) {
        constexpr int Q = decltype(QC_)::value;
#pragma unroll
        for (int d = 0; d < 4; ++d) {
            if (d != Q) {
#pragma unroll
                for (int i = 0; i < 8; ++i) rx[(((tt3 * 4 + d) * 4 + Q) * 8 + i) * 64 + lane] = (d >> 1) ? acc1[8 * (d & 1) + i] : acc0[8 * (d & 1) + i];
            } else {
#pragma unroll
                for (int i = 0; i < 8; ++i) v[i] = (d >> 1) ? acc1[8 * (d & 1) + i] : acc0[8 * (d & 1) + i];
            }
        }
    };
    if (q3 == 0) scatter(IC<0>{}); else if (q3 == 1) scatter(IC<1>{}); else if (q3 == 2) scatter(IC<2>{}); else scatter(IC<3>{});
    BAR();
#pragma unroll
    for (int s = 1; s < 4; ++s) {
        const unsigned sq = (q3 + s) & 3;
#pragma unroll
        for (int i = 0; i < 8; ++i) v[i] += rx[(((tt3 * 4 + q3) * 4 + sq) * 8 + i) * 64 + lane];
    }
#pragma unroll
    for (int gq = 0; gq < 2; ++gq) {
        const unsigned n = n0 + 8 * gq + 4 * h;
        const float y0 = v[4 * gq] + __uint_as_float(xr[gq][0] << 16) + __uint_as_float(br[gq][0] << 16), y1 = v[4 * gq + 1] + __uint_as_float(xr[gq][0] & 0xffff0000u) + __uint_as_float(br[gq][0] & 0xffff0000u);
        const float y2 = v[4 * gq + 2] + __uint_as_float(xr[gq][1] << 16) + __uint_as_float(br[gq][1] << 16), y3 = v[4 * gq + 3] + __uint_as_float(xr[gq][1] & 0xffff0000u) + __uint_as_float(br[gq][1] & 0xffff0000u);
        *(u32x2*)((char*)P.out + (tok * DM + n) * 2) = u32x2{cvtpk(y0, y1), cvtpk(y2, y3)};
    }
    STAMP(14);
}

__global__ __launch_bounds__(512, 2) void fused_kernel(const void* x, const void* wqkv, const void* wo, const void* w1, const void* b1, const void* w2, const void* b2, void* out, char* ws) {
    extern __shared__ __attribute__((aligned(16))) char lds_raw[];
    LAS char* lds = (LAS char*)lds_raw;
    const Ptrs P{x, wqkv, wo, w1, b1, w2, b2, out, ws};
    STAMP(0);
    u32x4 qf0, qf1; float kn2; P3W W3;
    const u64 tag = phase1(P, lds, blockIdx.x, qf0, qf1, kn2, W3);
    phase23(P, lds, blockIdx.x, tag, qf0, qf1, kn2, W3);
}

extern "C" void kernel_launch(void* const* d_in, const int* in_sizes, int n_in, void* d_out, int out_size, void* d_ws, size_t ws_size, hipStream_t stream) {
    (void)in_sizes; (void)n_in; (void)out_size; (void)ws_size;
    (void)hipFuncSetAttribute((const void*)fused_kernel, hipFuncAttributeMaxDynamicSharedMemorySize, LDS_BYTES);
    fused_kernel<<<256, 512, LDS_BYTES, stream>>>(d_in[0], d_in[1], d_in[2], d_in[4], d_in[5], d_in[6], d_in[7], d_out, (char*)d_ws);
}
```

```cpp
#include <hip/hip_runtime.h>
#include <hip/hip_bf16.h>
#include <stdint.h>
#include <type_traits>
#include <utility>

#define DI __device__ __forceinline__
#define LAS __attribute__((address_space(3)))
#define GAS __attribute__((address_space(1)))
typedef float f32x16 __attribute__((ext_vector_type(16)));
typedef float f32x4 __attribute__((ext_vector_type(4)));
typedef float f32x2 __attribute__((ext_vector_type(2)));
typedef __bf16 bf16x8 __attribute__((ext_vector_type(8)));
typedef __bf16 bf16x2 __attribute__((ext_vector_type(2)));
typedef unsigned u32x4 __attribute__((ext_vector_type(4)));
typedef unsigned u32x2 __attribute__((ext_vector_type(2)));
typedef short s16x4 __attribute__((ext_vector_type(4)));
typedef unsigned long long u64;
typedef __amdgpu_buffer_rsrc_t rsrc_t;

constexpr int SEQ = 2048, DM = 448, NH = 16, DH = 28, FF = 1792;
constexpr unsigned WS_EPOCH = 0, WS_FQKV = 4096, WS_FH = WS_FQKV + 256 * 64, WS_FO = WS_FH + 256 * 64;
constexpr unsigned WS_KN2 = 512u << 10;
constexpr unsigned WS_Q = 1u << 20, WS_K = 3u << 20, WS_V = 5u << 20, WS_O = 7u << 20, WS_H = 9u << 20;
constexpr unsigned WS_BYTES = 17u << 20;
constexpr float C2 = 0.27264234f;
constexpr float LN_EPS = 1e-5f;
constexpr int AUX_SC1 = 16;
constexpr unsigned LDS_BYTES = 160 * 1024;
constexpr unsigned SPIN_MAX = 200000;
constexpr unsigned OOB = 0x7ffffff0u;
#define SAFE_BOUND 40.f
#define QD 1
#define QB 1
#define KQC 1
#define QX 1
#define QPF 3
#define PUBS 18
#define OUT_AUX 0
#define XL 1
#define XB 1
#define XA 1

DI rsrc_t mk_rsrc(const void* p, unsigned bytes) { return __builtin_amdgcn_make_buffer_rsrc((void*)p, (short)0, (int)bytes, 0x00020000); }
DI unsigned rfl(unsigned x) { return __builtin_amdgcn_readfirstlane(x); }
DI unsigned rfl_pad(unsigned x) { unsigned r = rfl(x); asm volatile("s_nop 4" : "+s"(r)); return r; }
DI void dma16(rsrc_t rs, unsigned voff, unsigned soff, unsigned ldsaddr) {
    asm volatile("s_mov_b32 m0, %0\n\ts_nop 0\n\tbuffer_load_dwordx4 %1, %2, %3 offen lds" ::"s"(ldsaddr), "v"(voff), "s"(rs), "s"(soff) : "memory");
}
DI void dma16_sc1(rsrc_t rs, unsigned voff, unsigned soff, unsigned ldsaddr) {
    asm volatile("s_mov_b32 m0, %0\n\ts_nop 0\n\tbuffer_load_dwordx4 %1, %2, %3 offen sc1 lds" ::"s"(ldsaddr), "v"(voff), "s"(rs), "s"(soff) : "memory");
}
#define WAIT_VM0() asm volatile("s_waitcnt vmcnt(0)" ::: "memory")
#define BAR() asm volatile("s_waitcnt lgkmcnt(0)\n\ts_barrier" ::: "memory")
#define VM0_BAR() asm volatile("s_waitcnt vmcnt(0) lgkmcnt(0)\n\ts_barrier" ::: "memory")
#define VMCNT(n) asm volatile("s_waitcnt vmcnt(%0)" ::"n"(n) : "memory")
#define VMCNT_BAR(n) asm volatile("s_waitcnt vmcnt(%0) lgkmcnt(0)\n\ts_barrier" ::"n"(n) : "memory")
#define SBAR() __builtin_amdgcn_sched_barrier(0)
#define PIN(x) asm volatile("" : "+v"(x))

template <int V> using IC = std::integral_constant<int, V>;
template <int... Is, class F> DI void static_for_(std::integer_sequence<int, Is...>, F&& f) { (f(IC<Is>{}), ...); }
template <int N, class F> DI void static_for(F&& f) { static_for_(std::make_integer_sequence<int, N>{}, f); }

DI f32x16 mfma16(u32x4 a, u32x4 b, f32x16 c) { return __builtin_amdgcn_mfma_f32_32x32x16_bf16(__builtin_bit_cast(bf16x8, a), __builtin_bit_cast(bf16x8, b), c, 0, 0, 0); }
DI f32x16 mfma_f32(float a, float b, f32x16 c) { return __builtin_amdgcn_mfma_f32_32x32x2f32(a, b, c, 0, 0, 0); }
DI unsigned cvtpk(float lo, float hi) { f32x2 v = {lo, hi}; return __builtin_bit_cast(unsigned, __builtin_convertvector(v, bf16x2)); }
DI float dot2(unsigned a, unsigned b, float c) { return __builtin_amdgcn_fdot2_f32_bf16(__builtin_bit_cast(bf16x2, a), __builtin_bit_cast(bf16x2, b), c, false); }
DI float swap_add(float x) { auto r = __builtin_amdgcn_permlane32_swap(__float_as_uint(x), __float_as_uint(x), false, false); return __uint_as_float(r[0]) + __uint_as_float(r[1]); }
DI float swap_max(float x) { auto r = __builtin_amdgcn_permlane32_swap(__float_as_uint(x), __float_as_uint(x), false, false); return fmaxf(__uint_as_float(r[0]), __uint_as_float(r[1])); }
template <int CTRL> DI float dpp_max(float x) { return fmaxf(x, __uint_as_float(__builtin_amdgcn_update_dpp(0u, __float_as_uint(x), CTRL, 0xf, 0xf, true))); }
DI float max32(float x) {
    x = dpp_max<0x128>(x); x = dpp_max<0x124>(x); x = dpp_max<0x122>(x); x = dpp_max<0x121>(x);
    auto sw = __builtin_amdgcn_permlane16_swap(__float_as_uint(x), __float_as_uint(x), false, false);
    return fmaxf(__uint_as_float(sw[0]), __uint_as_float(sw[1]));
}
template <int D2> DI u32x4 tr_frag(LAS char* p) {
    s16x4 lo = __builtin_amdgcn_ds_read_tr16_b64_v4i16((LAS s16x4*)p), hi = __builtin_amdgcn_ds_read_tr16_b64_v4i16((LAS s16x4*)(p + D2));
    u32x2 a = __builtin_bit_cast(u32x2, lo), b = __builtin_bit_cast(u32x2, hi);
    return u32x4{a[0], a[1], b[0], b[1]};
}
DI u32x4 pack8(const f32x16& v, int s) { return u32x4{cvtpk(v[8 * s], v[8 * s + 1]), cvtpk(v[8 * s + 2], v[8 * s + 3]), cvtpk(v[8 * s + 4], v[8 * s + 5]), cvtpk(v[8 * s + 6], v[8 * s + 7])}; }
DI void st16(rsrc_t rs, unsigned off, u32x4 v) { __builtin_amdgcn_raw_buffer_store_b128(v, rs, off, 0, AUX_SC1); }
DI u32x4 ld16(rsrc_t rs, unsigned voff, unsigned soff) { return __builtin_amdgcn_raw_buffer_load_b128(rs, voff, soff, AUX_SC1); }
DI float bf2f(unsigned short b) { return __uint_as_float((unsigned)b << 16); }
DI u64 ld_flag(const char* p) { return __hip_atomic_load((GAS const u64*)p, __ATOMIC_RELAXED, __HIP_MEMORY_SCOPE_AGENT); }
DI void st_flag(char* p, u64 v) { __hip_atomic_store((GAS u64*)p, v, __ATOMIC_RELAXED, __HIP_MEMORY_SCOPE_AGENT); }
DI void poll_flags(const char* p, bool use, u64 tag) {
    for (unsigned n = 0; n < SPIN_MAX; ++n) {
        const u64 v = use ? ld_flag(p) : tag;
        if (__all(v == tag)) break;
        __builtin_amdgcn_s_sleep(1);
    }
    asm volatile("" ::: "memory");
}
template <class T> DI T* opaque(T* p) { asm volatile("" : "+v"(p)); return p; }
#define LDF(p) (*(LAS u32x4*)(p))

#define STAMP(i) do {} while (0)
#define FSTAMP(i) do {} while (0)
struct Ptrs { const void* x; const void* wqkv; const void* wo; const void* w1; const void* b1; const void* w2; const void* b2; void* out; char* ws; };

constexpr unsigned L_STAT = 155648, L_CS = 156672;
DI void sum8(const u32x4& f, float& a, float& b) {
    float t0, t1, t2, t3;
    asm volatile("v_lshlrev_b32 %2, 16, %6\n\tv_and_b32 %3, 0xffff0000, %6\n\tv_lshlrev_b32 %4, 16, %7\n\tv_and_b32 %5, 0xffff0000, %7\n\t"
                 "v_add_f32 %0, %0, %2\n\tv_add_f32 %1, %1, %3\n\tv_add_f32 %0, %0, %4\n\tv_add_f32 %1, %1, %5\n\t"
                 "v_lshlrev_b32 %2, 16, %8\n\tv_and_b32 %3, 0xffff0000, %8\n\tv_lshlrev_b32 %4, 16, %9\n\tv_and_b32 %5, 0xffff0000, %9\n\t"
                 "v_add_f32 %0, %0, %2\n\tv_add_f32 %1, %1, %3\n\tv_add_f32 %0, %0, %4\n\tv_add_f32 %1, %1, %5"
                 : "+v"(a), "+v"(b), "=&v"(t0), "=&v"(t1), "=&v"(t2), "=&v"(t3) : "v"(f[0]), "v"(f[1]), "v"(f[2]), "v"(f[3]));
}
DI void gelu_a(float a0, float a1, float rstd, float c0, float& x0, float& x1, float& e0, float& e1) {
    asm volatile("v_mul_f32 %0, %4, %6\n\tv_mul_f32 %1, %5, %6\n\tv_mul_f32 %2, %0, %0\n\tv_mul_f32 %3, %1, %1\n\t"
                 "v_fmamk_f32 %2, %2, 0x3dd2d3e8, %7\n\tv_fmamk_f32 %3, %3, 0x3dd2d3e8, %7\n\tv_mul_f32 %2, %0, %2\n\tv_mul_f32 %3, %1, %3\n\t"
                 "v_exp_f32 %2, -%2\n\tv_exp_f32 %3, -%3"
                 : "=&v"(x0), "=&v"(x1), "=&v"(e0), "=&v"(e1) : "v"(a0), "v"(a1), "v"(rstd), "v"(c0));
}
DI unsigned gelu_b(float x0, float x1, float e0, float e1) {
    unsigned r;
    asm volatile("v_add_f32 %1, 1.0, %1\n\tv_add_f32 %2, 1.0, %2\n\tv_rcp_f32 %1, %1\n\tv_rcp_f32 %2, %2\n\ts_nop 0\n\tv_mul_f32 %1, %3, %1\n\tv_mul_f32 %2, %4, %2\n\tv_cvt_pk_bf16_f32 %0, %1, %2"
                 : "=v"(r), "+v"(e0), "+v"(e1) : "v"(x0), "v"(x1));
    return r;
}
#define MFMA_F32_PAD(x) asm volatile("s_nop 15\n\ts_nop 3" : "+v"(x))
DI float fmul_s(float a, float b) { float r; asm("v_mul_f32 %0, %1, %2" : "=v"(r) : "v"(a), "v"(b)); return r; }
DI float ffma_s(float a, float b, float c) { float r; asm("v_fma_f32 %0, %1, %2, %3" : "=v"(r) : "v"(a), "v"(b), "v"(c)); return r; }
DI float fadd_s(float a, float b) { float r; asm("v_add_f32 %0, %1, %2" : "=v"(r) : "v"(a), "v"(b)); return r; }
struct P1Dma { rsrc_t rqkv, rw1, rw2, rwo; unsigned vq0, vq1, vq2, vf, qsoff, qdst, fsoff, fdst, tsoff, tdst, usoff, udst, tdummy, vt2, vto; };

struct QkvAddr { LAS char* qa[4]; LAS char* qa2[4]; LAS char* xu[4]; };
template <int HALF, bool SWA, bool SWB>
DI void p1_qkv_pass(const Ptrs& P, const P1Dma& D, u32x4 (&xf)[28], const QkvAddr& A, bool is28, f32x16& accA, f32x16& accB, float& csA, float& csB) {
    constexpr int PF = 3;
    u32x4 wa[PF], wb[PF];
    float a0 = 0.f, a1 = 0.f, b0 = 0.f, b1 = 0.f;
    const unsigned ones = 0x3F803F80u;
    accA = f32x16{}; accB = f32x16{};
    auto rd = [&](auto SC) {
        constexpr int s = decltype(SC)::value; constexpr int o = s < 12 ? (s & ~3) * 3072 : ((s & ~3) - 12) * 3072;
        wa[s % PF] = tr_frag<768>((s < 12 ? A.qa[s & 3] : A.qa2[s & 3]) + o); if (HALF == 0) wb[s % PF] = tr_frag<768>((s < 12 ? A.qa[s & 3] : A.qa2[s & 3]) + o + 56);
    };
    FSTAMP(16);
    rd(IC<0>{}); rd(IC<1>{}); rd(IC<2>{});
    SBAR();
    static_for<28>([&](auto SC) {
        constexpr int s = decltype(SC)::value;
        if constexpr (s == 20) FSTAMP(25);
        u32x4 ca = wa[s % PF]; const u32x4 cb = wb[s % PF];
        if (HALF == 1) { ca[0] = is28 ? ones : ca[0]; ca[1] = is28 ? ones : ca[1]; ca[2] = is28 ? ones : ca[2]; ca[3] = is28 ? ones : ca[3]; }
        accA = SWA ? mfma16(xf[s], ca, accA) : mfma16(ca, xf[s], accA);
        if constexpr (!QD) {} else if constexpr (s < 6) dma16(D.rqkv, s % 3 == 0 ? D.vq0 : s % 3 == 1 ? D.vq1 : D.vq2, D.qsoff + (s / 3) * (16 * 2688), D.qdst + s * 1024);
        else if constexpr (s >= 8 && s < 16) dma16(D.rw1, D.vf, D.fsoff + (s - 8) * (4 * 3584), D.fdst + (s - 8) * 1024);
        else if constexpr (s >= 16 && s < 20) dma16(D.rw1, D.vf, D.tsoff + (s - 16) * (4 * 3584), D.tdst + (s - 16) * 1024);
        if (HALF == 0) accB = SWB ? mfma16(xf[s], cb, accB) : mfma16(cb, xf[s], accB);
        else accB = mfma16(xf[s], xf[s], accB);
        if constexpr (s + PF < 28 && !(s + PF >= 12 && s <= 11)) rd(IC<(s + PF < 28 ? s + PF : 0)>{});
        if constexpr (QX && s >= 4 && s < 8) {
#pragma unroll
            for (int i = 0; i < 3; ++i) { constexpr int dummy = 0; (void)dummy; const int j = 3 * (s - 4) + i; xf[16 + j] = LDF(A.xu[j & 3] + 128 * (j >> 2)); }
        }
        if constexpr (KQC && (s & 3) == 0) { sum8(ca, a0, a1); if (HALF == 0) sum8(cb, b0, b1); }
        SBAR();
        if constexpr (s == 12) {
            asm volatile("s_mov_b32 m0, %0\n\ts_nop 0\n\tbuffer_load_dword %1, %2, 0 offen lds\n\tbuffer_load_dword %3, %4, 0 offen lds" ::"s"(D.tdummy), "v"(D.vt2), "s"(D.rw2), "v"(D.vto), "s"(D.rwo) : "memory");
        }
        if constexpr (QB && s == 3) { VMCNT_BAR(6); STAMP(18); SBAR(); }
        if constexpr (QB && s == 7) { FSTAMP(19); BAR(); FSTAMP(20); STAMP(19); SBAR(); }
        if constexpr (s == 11) {
            FSTAMP(21); if (QB) VMCNT_BAR(4); FSTAMP(24); STAMP(20);
            rd(IC<12>{}); rd(IC<13>{}); rd(IC<14>{});
            SBAR();
        }
    });
    csA = a0 + a1; csB = b0 + b1;
}
DI void p1_ffn_pass(const Ptrs& P, LAS char* lds, const P1Dma& D, const u32x4 (&xf)[28], unsigned tt, unsigned off, f32x16& accA, f32x16& accB, float& csA, float& csB,
                    char* flag, u64 tag) {
    LAS char* q[4]; LAS char* q2[4]; LAS char* p[4]; LAS char* p2[4];
#pragma unroll
    for (int u = 0; u < 4; ++u) {
        const unsigned ro = off + ((u + tt) & 3) * 4096;
        q[u] = opaque(lds + 49152 + ro); q2[u] = opaque(lds + 114688 + ro); p[u] = opaque(lds + 49152 + (ro ^ 64)); p2[u] = opaque(lds + 114688 + (ro ^ 64));
    }
    constexpr int PF = 3;
    u32x4 wa[PF], wb[PF];
    float a0 = 0.f, a1 = 0.f, b0 = 0.f, b1 = 0.f;
    accA = f32x16{}; accB = f32x16{};
    auto rd = [&](auto SC) {
        constexpr int s = decltype(SC)::value;
        if constexpr (s < 16) { wa[s % PF] = tr_frag<1024>(q[s & 3] + (s & ~3) * 4096); wb[s % PF] = tr_frag<1024>(p[s & 3] + (s & ~3) * 4096); }
        else if constexpr (s < 24) { wa[s % PF] = tr_frag<1024>(q2[s & 3] + ((s & ~3) - 16) * 4096); wb[s % PF] = tr_frag<1024>(p2[s & 3] + ((s & ~3) - 16) * 4096); }
        else { wa[s % PF] = tr_frag<1024>(q[s & 3] - 49152); wb[s % PF] = tr_frag<1024>(p[s & 3] - 49152); }
    };
    rd(IC<0>{}); rd(IC<1>{}); rd(IC<2>{});
    SBAR();
    static_for<28>([&](auto SC) {
        constexpr int s = decltype(SC)::value;
        const u32x4 ca = wa[s % PF], cb = wb[s % PF];
        accA = mfma16(ca, xf[s], accA);
        accB = mfma16(cb, xf[s], accB);
        if constexpr (s + PF < 28) rd(IC<(s + PF < 28 ? s + PF : 0)>{});
        if constexpr ((s & 3) == 0) sum8(ca, a0, a1);
        if constexpr ((s & 3) == 0) sum8(cb, b0, b1);
        SBAR();
        if constexpr (s == 12) {
            VMCNT_BAR(8);
            STAMP(21);
            SBAR();
        }
        if constexpr (s == PUBS) {
            VMCNT_BAR(0);
            if (threadIdx.x == 0) st_flag(flag, tag);
            SBAR();
        }
    });
    csA = a0 + a1; csB = b0 + b1;
}
DI float p1_cs(LAS float* csb, unsigned l31) {
    float v[8];
#pragma unroll
    for (int i = 0; i < 8; ++i) v[i] = csb[i * 32 + l31];
    return ((v[0] + v[1]) + (v[2] + v[3])) + ((v[4] + v[5]) + (v[6] + v[7]));
}
struct P1Ctx { rsrc_t rws; char* ws; unsigned lane, l31, h, tile; float mu, sig, rstd; };
DI void p1_gelu(const P1Ctx& C, f32x16 acc, float cs, float bias, u32x4& pk0, u32x4& pk1) {
    acc = mfma_f32(C.h ? bias : cs, C.h ? C.sig : -C.mu, acc);
#pragma unroll
    for (int i = 0; i < 16; i += 2) {
        const f32x2 xv = f32x2{acc[i], acc[i + 1]} * C.rstd, t2 = xv * xv;
        const f32x2 pl = __builtin_elementwise_fma(t2, f32x2{0.1029432397f, 0.1029432397f}, f32x2{2.302208198f, 2.302208198f}), z = xv * pl;
        const f32x2 d = f32x2{__builtin_amdgcn_exp2f(-z[0]), __builtin_amdgcn_exp2f(-z[1])} + 1.f;
        const f32x2 y = xv * f32x2{__builtin_amdgcn_rcpf(d[0]), __builtin_amdgcn_rcpf(d[1])};
        acc[i] = y[0]; acc[i + 1] = y[1];
    }
    pk0 = pack8(acc, 0); pk1 = pack8(acc, 1);
}
DI void p1_store_h(const P1Ctx& C, unsigned ft, u32x4 pk0, u32x4 pk1) {
    const unsigned off = WS_H + (C.tile * 112 + 2 * ft) * 1024 + C.lane * 16;
    st16(C.rws, off, pk0); st16(C.rws, off + 1024, pk1);
}
DI void p1_epi_qkv(const P1Ctx& C, f32x16 acc, float cs, unsigned type, unsigned head) {
    const unsigned h = C.h, l31 = C.l31;
    const bool isv = type == 2;
    const float m0 = h ? 0.f : -C.mu, c0 = h ? 0.f : cs, r0 = h ? 0.f : (type == 0 ? C.rstd * C2 : C.rstd), k0 = (h || l31 >= 28) ? 0.f : 1.f;
    acc = mfma_f32(isv ? m0 : c0, isv ? c0 : m0, acc);
    f32x16 R = {}; R = mfma_f32(isv ? r0 : k0, isv ? k0 : r0, R);
    const float pv = (isv && l31 == 28) ? 1.f : 0.f, p12 = isv ? pv : ((type == 1 && h) ? 1.f : 0.f);
    f32x2 n2 = {0.f, 0.f};
#pragma unroll
    for (int i = 0; i < 16; i += 2) {
        const f32x2 t = __builtin_elementwise_fma(f32x2{acc[i], acc[i + 1]}, f32x2{R[i], R[i + 1]}, f32x2{i == 12 ? p12 : pv, pv});
        acc[i] = t[0]; acc[i + 1] = t[1];
        n2 = __builtin_elementwise_fma(t, t, n2);
    }
    const unsigned off = WS_Q + type * (2u << 20) + (head * 64 + C.tile) * 2048 + C.lane * 16;
    st16(C.rws, off, pack8(acc, 0)); st16(C.rws, off + 1024, pack8(acc, 1));
    const float kn = max32(swap_add(n2[0] + n2[1]));
    __builtin_amdgcn_raw_buffer_store_b32(__float_as_uint(kn), C.rws, (type == 1 && C.lane == 0) ? WS_KN2 + (head * 64 + C.tile) * 4 : OOB, 0, AUX_SC1);
}

DI u64 make_tag(u64 e) {
    e = ((u64)rfl((unsigned)(e >> 32)) << 32) | rfl((unsigned)e);
    u64 tag = (e + 0x9E3779B97F4A7C15ull); tag = (tag ^ (tag >> 30)) * 0xBF58476D1CE4E5B9ull; tag = (tag ^ (tag >> 27)) * 0x94D049BB133111EBull; tag ^= tag >> 31;
    return tag;
}
constexpr unsigned p3_region(int sb) { return (sb % 3) * 49152u; }
constexpr unsigned RING = 98304;
constexpr unsigned ring_off(int c) { return c % 4 == 2 ? 0u : c % 4 == 0 ? 16384u : c % 4 == 1 ? 32768u : 49152u; }
struct P3W { rsrc_t rw2, rwo; unsigned vw, vo0, vo1, soff, dst, hd0, vt2, vto; };
template <int SB, int I> DI void p3_dma(const P3W& W) {
    constexpr int j = 6 * SB + I;
    if constexpr (j < 28) dma16(W.rw2, W.vw, W.soff + j * (16 * DM * 2), W.dst + p3_region(SB) + I * 2048);
    else {
        dma16(W.rwo, ((j - 28) & 1) ? W.vo1 : W.vo0, W.hd0 + ((j - 28) >> 1) * (28 * DM * 2), W.dst + p3_region(SB) + I * 2048);
    }
}
DI P3W make_p3w(const Ptrs& P, unsigned lds0, int b, unsigned w, unsigned lane) {
    const unsigned r = b >> 3, cg3 = r < 28 ? r >> 2 : 0;
    P3W W3;
    const unsigned vcol = cg3 * 128 + (((lane >> 2) & 1) ^ ((lane >> 4) & 1)) * 64 + (lane & 3) * 16, rw = (w & 1) * 8 + (lane >> 3);
    W3.rw2 = mk_rsrc(P.w2, FF * DM * 2); W3.rwo = mk_rsrc(P.wo, DM * DM * 2);
    W3.vw = rw * (DM * 2) + vcol; W3.vo0 = W3.vw; W3.vo1 = W3.vw + 16 * (DM * 2);
    W3.soff = rfl_pad(28 * (w >> 1) * (16 * DM * 2)); W3.hd0 = rfl_pad(4 * (w >> 1) * (28 * DM * 2));
    W3.dst = rfl_pad(lds0 + (w >> 1) * 6 * 2048 + (w & 1) * 1024);
    {
        const unsigned L = w * 64 + lane, j = 12 + (L >> 4), row = L & 15, qq = r & 3;
        W3.vt2 = w < 4 ? ((28 * qq + j) * 16 + row) * (DM * 2) + cg3 * 128 : OOB;
        W3.vto = (w == 4 || w == 5) ? (28 * (4 * qq + ((j - 28) >> 1)) + 16 * ((j - 28) & 1) + row) * (DM * 2) + cg3 * 128 : OOB;
    }
    return W3;
}
DI void kv_first(const Ptrs& P, LAS char* lds, int b, unsigned w, unsigned lane, u32x4& qf0, u32x4& qf1, float& kn2) {
    const unsigned g = b & 7, r = b >> 3, head = 2 * g + (r >> 4), tile = (r & 15) * 4 + (w & 3);
    const rsrc_t rws = mk_rsrc(P.ws, WS_BYTES);
    const unsigned src = rfl_pad(((w & 1) ? WS_V : WS_K) + (head * 64 + (w >> 2) * 32 + ((w >> 1) & 1)) * 2048), wdst = rfl_pad((unsigned)(size_t)lds + 98304 + w * 2048);
    dma16_sc1(rws, lane * 16, src, wdst + 16384); dma16_sc1(rws, lane * 16 + 1024, src, wdst + 16384 + 1024);
    dma16_sc1(rws, lane * 16, src + 4096, wdst + 32768); dma16_sc1(rws, lane * 16 + 1024, src + 4096, wdst + 32768 + 1024);
    const unsigned qs = rfl_pad(WS_Q + (head * 64 + tile) * 2048), ks = rfl_pad(WS_KN2 + head * 256);
    asm volatile("buffer_load_dwordx4 %0, %3, %5, %6 offen sc1\n\tbuffer_load_dwordx4 %1, %3, %5, %6 offen offset:1024 sc1\n\tbuffer_load_dword %2, %4, %5, %7 offen sc1"
                 : "=&v"(qf0), "=&v"(qf1), "=&v"(kn2) : "v"(lane * 16), "v"(lane * 4), "s"(rws), "s"(qs), "s"(ks) : "memory");
}
DI u64 phase1(const Ptrs& P, LAS char* lds, int b, u32x4& qf0, u32x4& qf1, float& kn2, P3W& W3) {
    const unsigned tid = threadIdx.x, lane = tid & 63, l31 = lane & 31, h = lane >> 5;
    const unsigned w = rfl(tid >> 6), tt = w & 3, half = w >> 2;
    const unsigned g = b & 7, r = b >> 3, tb = 4 * (g >> 1) + (r & 3), cg = 8 * (g & 1) + (r >> 2);
    const unsigned lds0 = (unsigned)(size_t)lds;
    const rsrc_t rx = mk_rsrc(P.x, SEQ * DM * 2);
    P1Dma D;
    D.rqkv = mk_rsrc(P.wqkv, DM * 3 * DM * 2);
    const unsigned cb = 168 * cg, cb16 = cb & ~15u, dl = cb & 15u;
    FSTAMP(13);
    {
        const unsigned rowa = 2 * w + h;
        const unsigned va = rowa * 896 + ((l31 ^ (rowa & 15)) << 4), sa = tb * 128 * 896;
#pragma unroll
        for (unsigned i = 0; i < 8; ++i) dma16(rx, va, sa + i * (16 * 896), lds0 + (w + 8 * i) * 1024);
    }
    {
        auto vq = [&](unsigned p) { const unsigned row = p / 12, ch = p - row * 12; return ch < 11 ? row * 2688 + cb16 + ch * 16 : OOB; };
        D.vq0 = vq(lane); D.vq1 = vq(64 + lane); D.vq2 = vq(128 + lane);
    }
    {
        const unsigned so = w * (16 * 2688), ds = lds0 + 114688 + w * 3072;
        dma16(D.rqkv, D.vq0, so, ds); dma16(D.rqkv, D.vq1, so, ds + 1024); dma16(D.rqkv, D.vq2, so, ds + 2048);
        if (w < 4) { dma16(D.rqkv, D.vq0, so + 8 * (16 * 2688), ds + 8 * 3072); dma16(D.rqkv, D.vq1, so + 8 * (16 * 2688), ds + 8 * 3072 + 1024); dma16(D.rqkv, D.vq2, so + 8 * (16 * 2688), ds + 8 * 3072 + 2048); }
    }
    const unsigned ntile = ((cg ^ (cg >> 1)) & 1) ? 3 : 4, fstart = 7 * (cg >> 1) + ((cg & 1) ? (((cg >> 1) & 1) ? 3 : 4) : 0);
    D.rw1 = mk_rsrc(P.w1, DM * FF * 2);
    { const unsigned rq = lane >> 4, sl = ((lane >> 2) & 3) ^ rq; D.vf = sl < ntile ? rq * 3584 + (fstart + sl) * 64 + (lane & 3) * 16 : OOB; }
    D.qsoff = (12 + 2 * w) * (16 * 2688); D.qdst = lds0 + 2 * w * 3072;
    D.fsoff = 2 * w * (16 * 3584); D.fdst = lds0 + 49152 + 2 * w * 4096;
    D.tsoff = (256 + 16 * w) * 3584; D.tdst = lds0 + 114688 + w * 4096;
    D.usoff = (384 + 8 * w) * 3584; D.udst = lds0 + 2 * w * 1024;
    D.rw2 = mk_rsrc(P.w2, FF * DM * 2); D.rwo = mk_rsrc(P.wo, DM * DM * 2); D.tdummy = lds0 + 151552 + w * 256;
    { const unsigned i = w * 8 + (lane & 7); D.vt2 = (lane < 8 && i < 49) ? (b * 49 + i) * 128 : OOB; D.vto = (lane < 8 && i < 13) ? (b * 13 + i) * 128 : OOB; }
    const u64 eraw = ld_flag(P.ws + WS_EPOCH);
    const unsigned short b1a = ((const unsigned short*)P.b1)[(fstart + 2 * half) * 32 + l31];
    const unsigned short b1b = ((const unsigned short*)P.b1)[(2 * half + 1 < ntile ? fstart + 2 * half + 1 : fstart) * 32 + l31];
#pragma unroll
    for (unsigned i = 0; i < 6; ++i) {
        const unsigned p = 64 * i + lane, rr = p / 24, c = p - rr * 24, row = 16 * w + rr;
        dma16(rx, row * 896 + 512 + ((c ^ ((row >> 1) & 7)) << 4), tb * 128 * 896, lds0 + 65536 + (6 * w + i) * 1024);
    }
    const rsrc_t rws = mk_rsrc(P.ws, WS_BYTES);
    LAS float* stat = (LAS float*)(lds + L_STAT);
    LAS float* csb = (LAS float*)(lds + L_CS);
    P1Ctx C{rws, P.ws, lane, l31, h, tb * 4 + tt, 0.f, 0.f, 0.f};
    const unsigned G = lane >> 4, q4 = (lane & 15) >> 2;
    const unsigned trq = (8 * (G >> 1) + q4) * 192 + dl + (G & 1) * 32 + (lane & 3) * 8 + half * 112;
    const unsigned trf = (8 * (G >> 1) + q4) * 256 + (G & 1) * 32 + (lane & 3) * 8;
    f32x16 accA, accB; float csA, csB;
    const unsigned tA = 3 * cg + 2 * half, tB = 3 * cg + 1, tyA = tA >> 4, tyB = tB >> 4;
    QkvAddr QA;
    {
        LAS char* xb0 = lds + 65536 + (tt * 32 + l31) * 384; const unsigned gswb = (l31 >> 1) & 7;
#pragma unroll
        for (int u = 0; u < 4; ++u) {
            const unsigned ro = ((u + tt) & 3) * 3072;
            QA.qa[u] = opaque(lds + 114688 + trq + ro); QA.qa2[u] = opaque(lds + trq + ro);
            QA.xu[u] = opaque(xb0 + (((2 * ((u + tt) & 3) + h) ^ gswb) << 4));
        }
    }
    const bool m28 = l31 == 28;
    {
        const unsigned i5 = w * 5 + lane, i7 = w * 7 + lane, g5 = b * 37 + i5;
        const unsigned v5 = (lane < 5 && i5 < 37 && g5 < 9408) ? g5 * 128 : OOB, v7 = (lane < 7 && i7 < 49) ? (b * 49 + i7) * 128 : OOB;
        asm volatile("s_mov_b32 m0, %0\n\ts_nop 0\n\tbuffer_load_dword %1, %2, 0 offen lds\n\tbuffer_load_dword %3, %4, 0 offen lds" ::"s"(D.tdummy), "v"(v5), "s"(D.rqkv), "v"(v7), "s"(D.rw1) : "memory");
    }
    FSTAMP(14);
    VMCNT_BAR(8);
    STAMP(1);
    u32x4 xf[28];
    {
        LAS char* xa = lds + (tt * 32 + l31) * 512;
#pragma unroll
        for (int u = 0; u < 4; ++u) {
            const unsigned c = 2 * ((u + tt) & 3) + h;
            LAS char* x0 = xa + ((c ^ (l31 & 15)) << 4); LAS char* x1 = xa + (((c + 8) ^ (l31 & 15)) << 4);
            xf[u] = LDF(x0); xf[4 + u] = LDF(x1); xf[8 + u] = LDF(x0 + 256); xf[12 + u] = LDF(x1 + 256);
        }
    }
    const u64 tag = make_tag(eraw);
    BAR();
    STAMP(2);
    if (half == 0) {
        if (tyA == 2) p1_qkv_pass<0, true, true>(P, D, xf, QA, m28, accA, accB, csA, csB);
        else if (tyB == 2) p1_qkv_pass<0, false, true>(P, D, xf, QA, m28, accA, accB, csA, csB);
        else p1_qkv_pass<0, false, false>(P, D, xf, QA, m28, accA, accB, csA, csB);
        csb[0 * 256 + tt * 64 + lane] = csA; csb[1 * 256 + tt * 64 + lane] = csB;
    } else {
        float a1;
        if (tyA == 2) {
            p1_qkv_pass<1, true, false>(P, D, xf, QA, m28, accA, accB, csA, csB);
            if (l31 == 28) {
#pragma unroll
                for (int i = 0; i < 16; ++i) stat[(tt * 32 + (i & 3) + 8 * (i >> 2) + 4 * h) * 2] = accA[i];
            }
            asm volatile("s_waitcnt lgkmcnt(0)" ::: "memory");
            a1 = stat[(tt * 32 + l31) * 2];
        } else {
            p1_qkv_pass<1, false, false>(P, D, xf, QA, m28, accA, accB, csA, csB);
            auto sw = __builtin_amdgcn_permlane32_swap(__float_as_uint(accA[12]), __float_as_uint(accA[12]), false, false);
            a1 = __uint_as_float(sw[1]);
        }
        csb[2 * 256 + tt * 64 + lane] = csA;
        const unsigned ri = (l31 & 3) + 4 * (l31 >> 3);
        float dg = 0.f;
#pragma unroll
        for (int i = 0; i < 16; ++i) dg = ri == (unsigned)i ? accB[i] : dg;
        const float a2 = swap_add(h == ((l31 >> 2) & 1) ? dg : 0.f);
        const float m = a1 * (1.f / DM), var = fmaxf(a2 * (1.f / DM) - m * m, 0.f) + LN_EPS;
        if (!h) *(LAS f32x2*)(stat + (tt * 32 + l31) * 2) = f32x2{m, __builtin_amdgcn_rsqf(var)};
    }
    FSTAMP(2);
    VMCNT_BAR(4);
    STAMP(3);
    dma16(D.rw1, D.vf, D.usoff, D.udst); dma16(D.rw1, D.vf, D.usoff + 4 * 3584, D.udst + 1024);
    { const f32x2 st = *(LAS f32x2*)(stat + (tt * 32 + l31) * 2); C.mu = st[0]; C.rstd = st[1]; C.sig = __builtin_amdgcn_rcpf(st[1]); }
    {
        const float c0 = p1_cs(csb + 2 * half * 256, l31), c1 = p1_cs(csb + 256, l31);
        p1_epi_qkv(C, accA, c0, tyA, tA & 15);
        if (half == 0) p1_epi_qkv(C, accB, c1, tyB, tB & 15);
        else asm volatile("buffer_store_dword %0, %0, %1, 0 offen sc1\n\tbuffer_store_dword %0, %0, %1, 0 offen sc1\n\tbuffer_store_dword %0, %0, %1, 0 offen sc1" ::"v"(OOB), "s"(rws) : "memory");
    }
    FSTAMP(5);
    STAMP(4);
    const unsigned head2 = 2 * g + (r >> 4), qb2 = r & 15;
    const char* qflag;
    { const unsigned cq = head2 / 3, ck = (16 + head2) / 3, cv = (32 + head2) / 3;
      const unsigned idx = lane < 16 ? ck * 16 + lane : lane < 32 ? cv * 16 + (lane - 16) : cq * 16 + qb2; qflag = P.ws + WS_FQKV + (lane <= 32 ? idx : 0) * 64; }
    p1_ffn_pass(P, lds, D, xf, tt, trf + 64 * ((2 * half) ^ q4), accA, accB, csA, csB, P.ws + WS_FQKV + (cg * 16 + tb) * 64, tag);
    csb[(3 + 2 * half) * 256 + tt * 64 + lane] = csA; csb[(4 + 2 * half) * 256 + tt * 64 + lane] = csB;
    W3 = make_p3w(P, lds0, b, w, lane);
    STAMP(22);
    BAR();
    STAMP(23);
    {
        const bool second = 2 * half + 1 < ntile;
        const float c0 = p1_cs(csb + (3 + 2 * half) * 256, l31), c1 = p1_cs(csb + (4 + 2 * half) * 256, l31);
        u32x4 pk0, pk1;
        FSTAMP(27);
        poll_flags(qflag, lane <= 32, tag);
        FSTAMP(28);
        kv_first(P, lds, b, w, lane, qf0, qf1, kn2);
        FSTAMP(29);
        p1_gelu(C, accA, c0, bf2f(b1a), pk0, pk1); p1_store_h(C, fstart + 2 * half, pk0, pk1);
        if (second) { p1_gelu(C, accB, c1, bf2f(b1b), pk0, pk1); p1_store_h(C, fstart + 2 * half + 1, pk0, pk1); }
        else { asm volatile("buffer_store_dword %0, %0, %1, 0 offen sc1\n\tbuffer_store_dword %0, %0, %1, 0 offen sc1" ::"v"(OOB), "s"(rws) : "memory"); }
    }
    FSTAMP(12);
    STAMP(6);
    return tag;
}

constexpr int att_aflo(int T) { return (!XA || T < 6) ? 0 : (T - 6) * 28 / 26; }
constexpr int att_kv(int T) { return (XL && (T & 1) == 0 && (T >> 1) + 3 < 16) ? 2 : 0; }
constexpr int att_w(int T) { return T < 12 ? 1 : T == 28 ? 2 : 0; }
constexpr int att_all(int T0, int T1) { int n = 0; for (int T = T0; T < T1; ++T) n += att_kv(T) + att_w(T) + (att_aflo(T + 1) - att_aflo(T)); return n; }
#define EXP4(S, i) do { S[i] = __builtin_amdgcn_exp2f(S[i]); S[i + 1] = __builtin_amdgcn_exp2f(S[i + 1]); S[i + 2] = __builtin_amdgcn_exp2f(S[i + 2]); S[i + 3] = __builtin_amdgcn_exp2f(S[i + 3]); } while (0)
template <int KH>
DI void att_fast(LAS char* lds, rsrc_t rws, const P3W& W3, unsigned lane, unsigned w, unsigned src, unsigned wdst, unsigned afsrc, u32x4 qf0, u32x4 qf1, f32x16& o, u32x4 (&af)[28], const char* hflag, char* myh, u64 tag, unsigned& tch0, unsigned& tch1) {
    LAS char* kvb = opaque(lds + RING + KH * 8192 + lane * 16);
    const unsigned vo = lane * 16, vo1 = lane * 16 + 1024;
    src = rfl_pad(src); wdst = rfl_pad(wdst); afsrc = rfl_pad(afsrc);
    const f32x16 zero = {};
    f32x16 sc[2];
    u32x4 k0 = LDF(kvb + ring_off(0)), k1 = LDF(kvb + ring_off(0) + 1024), v0 = LDF(kvb + ring_off(0) + 2048), v1 = LDF(kvb + ring_off(0) + 3072), p1 = {0u, 0u, 0u, 0u}, v1p = {0u, 0u, 0u, 0u};
    sc[0] = mfma16(k0, qf0, zero); sc[0] = mfma16(k1, qf1, sc[0]);
    k0 = LDF(kvb + ring_off(0) + 4096); k1 = LDF(kvb + ring_off(0) + 4096 + 1024);
    static_for<32>([&](auto TC) {
        constexpr int T = decltype(TC)::value, c = T >> 1;
        f32x16& S = sc[T & 1]; f32x16& SN = sc[(T + 1) & 1];
        constexpr int o1 = ring_off((T + 1) >> 1) + ((T + 1) & 1) * 4096, o2 = ring_off((T + 2) >> 1) + ((T + 2) & 1) * 4096;
        u32x4 p0, k0n, k1n, v0n, v1n;
        auto chunk_barrier = [&](auto OPS) {
            constexpr int younger = decltype(OPS)::value;
            if constexpr (T == 5 && KH == 0) { if (w == 0) { PIN(p1); PIN(o); poll_flags(hflag, lane < 16, tag); } }
            if (XB) asm volatile("s_waitcnt vmcnt(%0) lgkmcnt(%1)\n\ts_barrier" ::"n"(younger), "n"(KH == 0 ? 4 : 2) : "memory");
            if constexpr (T == 1 && KH == 0) { if (w == 0) { PIN(p1); PIN(o); if (lane == 0) st_flag(myh, tag); } }
        };
        S[0] = __builtin_amdgcn_exp2f(S[0]); S[1] = __builtin_amdgcn_exp2f(S[1]);
        if (T > 0) o = mfma16(v1p, p1, o);
        S[2] = __builtin_amdgcn_exp2f(S[2]); S[3] = __builtin_amdgcn_exp2f(S[3]);
        if (T + 2 < 32) { k0n = LDF(kvb + o2); k1n = LDF(kvb + o2 + 1024); }
        p0[0] = cvtpk(S[0], S[1]); p0[1] = cvtpk(S[2], S[3]); SBAR();
        if (T + 1 < 32) SN = mfma16(k0, qf0, zero);
        EXP4(S, 4);
        if constexpr (att_kv(T) != 0) { dma16_sc1(rws, vo, src + (c + 3) * 4096, wdst + ring_off(c + 3)); dma16_sc1(rws, vo1, src + (c + 3) * 4096, wdst + ring_off(c + 3) + 1024); }
        if constexpr (T < 12) p3_dma<T / 6, T % 6>(W3);
        if constexpr (T == 28) { tch0 = __builtin_amdgcn_raw_buffer_load_b32(W3.rw2, W3.vt2, 0, 0); tch1 = __builtin_amdgcn_raw_buffer_load_b32(W3.rwo, W3.vto, 0, 0); }
        static_for<att_aflo(T + 1) - att_aflo(T)>([&](auto JC) { constexpr int j = att_aflo(T) + decltype(JC)::value; af[j] = ld16(rws, vo + (j & 3) * 1024, afsrc + (j >> 2) * 4096); });
        p0[2] = cvtpk(S[4], S[5]); p0[3] = cvtpk(S[6], S[7]); SBAR();
        constexpr int younger = c >= 1 ? att_all(2 * c - 2, T + 1) - att_kv(2 * c - 2) : att_all(0, T + 1);
        if constexpr (KH == 1 && (T & 1) == 1 && T < 31) chunk_barrier(IC<younger>{});
        if (T + 1 < 32) SN = mfma16(k1, qf1, SN);
        EXP4(S, 8);
        if (T + 1 < 32) { v0n = LDF(kvb + o1 + 2048); v1n = LDF(kvb + o1 + 3072); }
        p1[0] = cvtpk(S[8], S[9]); p1[1] = cvtpk(S[10], S[11]); SBAR();
        o = mfma16(v0, p0, o);
        EXP4(S, 12);
        p1[2] = cvtpk(S[12], S[13]); p1[3] = cvtpk(S[14], S[15]); SBAR();
        v1p = v1;
        if (T + 2 < 32) { k0 = k0n; k1 = k1n; }
        if (T + 1 < 32) { v0 = v0n; v1 = v1n; }
        if constexpr (KH == 0 && (T & 1) == 1 && T < 31) chunk_barrier(IC<younger>{});
    });
    o = mfma16(v1p, p1, o);
}

DI void phase23(const Ptrs& P, LAS char* lds, int b, u64 tag, u32x4 qf0, u32x4 qf1, float kn2, const P3W& W3) {
    const unsigned tid = threadIdx.x, lane = tid & 63, l31 = lane & 31, h = lane >> 5;
    const unsigned w = rfl(tid >> 6), qt = w & 3, kh = w >> 2;
    const unsigned g = b & 7, r = b >> 3, head = 2 * g + (r >> 4), qb = r & 15, tile = qb * 4 + qt;
    const unsigned lds0 = (unsigned)(size_t)lds;
    const rsrc_t rws = mk_rsrc(P.ws, WS_BYTES);
    const bool p3 = r < 28;
    const unsigned tt3 = w & 1, q3 = w >> 1, tb3 = 4 * g + (r & 3), cg3 = p3 ? r >> 2 : 0, tile3 = tb3 * 2 + tt3;
    STAMP(7);
    const unsigned src = rfl_pad(((w & 1) ? WS_V : WS_K) + (head * 64 + kh * 32 + ((w >> 1) & 1)) * 2048);
    const unsigned wdst = rfl_pad(lds0 + RING + w * 2048);
#define STAGE(c, off) do { dma16_sc1(rws, lane * 16, src + (c) * 4096, wdst + (off)); dma16_sc1(rws, lane * 16 + 1024, src + (c) * 4096, wdst + (off) + 1024); } while (0)
    asm volatile("s_waitcnt vmcnt(4)" : "+v"(qf0), "+v"(qf1), "+v"(kn2)::"memory");
    kn2 = swap_max(max32(kn2));
    float qn2 = 0.f;
#pragma unroll
    for (int e = 0; e < 4; ++e) { qn2 = dot2(qf0[e], qf0[e], qn2); qn2 = dot2(qf1[e], qf1[e], qn2); }
    qn2 = swap_add(qn2);
    const float bound = __builtin_amdgcn_sqrtf(qn2 * kn2) * 1.01f + 0.01f;
    const bool safe = __all(bound <= SAFE_BOUND);
    float mrun;
    if (safe) {
        const unsigned nb = cvtpk(-bound, 0.f) & 0xffffu;
        if (h) qf1[2] = (qf1[2] & 0xffff0000u) | nb;
        mrun = __uint_as_float((nb & 0x7fffu) << 16);
    } else mrun = -INFINITY;
    PIN(qf1); PIN(mrun);
    STAGE(2, ring_off(2));
    BAR();
    char* myh = P.ws + WS_FH + ((4 * (g >> 1) + (r & 3)) * 16 + 8 * (g & 1) + (r >> 2)) * 64;
    STAMP(8);
    f32x16 o = {};
    u32x4 af[28];
    unsigned tch0 = 0, tch1 = 0;
    const unsigned afsrc = WS_H + (tile3 * 112 + 28 * q3) * 1024;
    const char* hflag = P.ws + WS_FH + ((tb3 >> 1) * 16 + (lane & 15)) * 64;
    if (safe) {
        if (kh == 0) att_fast<0>(lds, rws, W3, lane, w, src, wdst, afsrc, qf0, qf1, o, af, hflag, myh, tag, tch0, tch1);
        else att_fast<1>(lds, rws, W3, lane, w, src, wdst, afsrc, qf0, qf1, o, af, hflag, myh, tag, tch0, tch1);
    } else {
        const f32x16 zero = {};
        LAS char* kvb = lds + RING + kh * 8192 + lane * 16;
        static_for<12>([&](auto TC) { constexpr int T = decltype(TC)::value; p3_dma<T / 6, T % 6>(W3); });
#pragma unroll 1
        for (unsigned c = 0; c < 16; ++c) {
            auto roff = [](unsigned c) { return (c & 3) == 2 ? 0u : (c & 3) == 0 ? 16384u : (c & 3) == 1 ? 32768u : 49152u; };
            if (c + 3 < 16) STAGE(c + 3, roff(c + 3));
#pragma unroll 1
            for (int t = 0; t < 2; ++t) {
                LAS char* kv = kvb + roff(c) + t * 4096;
                const u32x4 k0 = LDF(kv), k1 = LDF(kv + 1024), v0 = LDF(kv + 2048), v1 = LDF(kv + 3072);
                f32x16 s = mfma16(k0, qf0, zero); s = mfma16(k1, qf1, s);
                float m = s[0];
#pragma unroll
                for (int i = 1; i < 16; ++i) m = fmaxf(m, s[i]);
                m = fmaxf(swap_max(m), mrun);
                const float f = __builtin_amdgcn_exp2f(mrun - m);
                mrun = m;
#pragma unroll
                for (int i = 0; i < 16; ++i) { s[i] = __builtin_amdgcn_exp2f(s[i] - m); o[i] *= f; }
                o = mfma16(v0, pack8(s, 0), o); o = mfma16(v1, pack8(s, 1), o);
            }
            if (c == 2 && w == 0) poll_flags(hflag, lane < 16, tag);
            if (c < 15) VM0_BAR();
            if (c == 0 && tid == 0) st_flag(myh, tag);
        }
#pragma unroll
        for (int j = 0; j < 28; ++j) af[j] = ld16(rws, lane * 16, afsrc + j * 1024);
    }
    BAR();
    STAMP(9);
    LAS float* ex = (LAS float*)(lds + 147456 + qt * 4096);
    if (kh == 1) {
        if (h) o[15] = mrun;
#pragma unroll
        for (int i = 0; i < 16; ++i) ex[i * 64 + lane] = o[i];
    }
    BAR();
    if (kh == 0) {
        const float m2 = ex[15 * 64 + 32 + l31], m = fmaxf(mrun, m2), f1 = __builtin_amdgcn_exp2f(mrun - m), f2 = __builtin_amdgcn_exp2f(m2 - m);
#pragma unroll
        for (int i = 0; i < 16; ++i) o[i] = o[i] * f1 + ex[i * 64 + lane] * f2;
        auto sw = __builtin_amdgcn_permlane32_swap(__float_as_uint(o[12]), __float_as_uint(o[12]), false, false);
        const float l = __uint_as_float(sw[1]);
        const float il = __builtin_amdgcn_rcpf(l);
#pragma unroll
        for (int i = 0; i < 16; ++i) o[i] *= il;
        if (h) { o[12] = 0.f; o[13] = 0.f; o[14] = 0.f; o[15] = 0.f; }
        const unsigned off = WS_O + (tile * 16 + head) * 2048 + lane * 16;
        st16(rws, off, pack8(o, 0)); st16(rws, off + 1024, pack8(o, 1));
    }
    STAMP(10);
    if (!p3) {
        VM0_BAR();
        if (tid == 0) st_flag(P.ws + WS_FO + (qb * 16 + head) * 64, tag);
        if (b == 255 && w == 0) {
#pragma unroll 1
            for (int i = 0; i < 4; ++i) poll_flags(P.ws + WS_FQKV + (i * 64 + lane) * 64, true, tag);
            if (lane == 0) st_flag(P.ws + WS_EPOCH, tag);
        }
        return;
    }
    const unsigned q4 = (lane & 15) >> 2, trb = (4 * (lane >> 5) + q4) * 128 + ((lane >> 4) & 1) * 32 + (lane & 3) * 8 + q3 * 12 * 1024;
    const unsigned tro0 = trb + ((q4 >> 1) & 1) * 64, tro1 = trb + (((q4 >> 1) & 1) ^ 1) * 64;
    const unsigned tok = tile3 * 32 + l31, n0 = cg3 * 64 + q3 * 16;
    u32x2 xr[2], br[2];
#pragma unroll
    for (int gq = 0; gq < 2; ++gq) {
        const unsigned n = n0 + 8 * gq + 4 * h;
        xr[gq] = *(const u32x2*)((const char*)P.x + (tok * DM + n) * 2); br[gq] = *(const u32x2*)((const char*)P.b2 + n * 2);
    }
    f32x16 acc0 = {}, acc1 = {};
    u32x4 ao[8];
    static_for<6>([&](auto SC) {
        constexpr int sb = decltype(SC)::value;
        LAS char* wp0 = opaque(lds + p3_region(sb) + tro0); LAS char* wp1 = opaque(lds + p3_region(sb) + tro1);
        u32x4 w0[3], w1[3];
#pragma unroll
        for (int i = 0; i < 3; ++i) { w0[i] = tr_frag<1024>(wp0 + i * 2048); w1[i] = tr_frag<1024>(wp1 + i * 2048); }
        if (sb == 3) {
#pragma unroll
            for (int j = 0; j < 8; ++j) ao[j] = ld16(rws, lane * 16 + (j & 3) * 1024, WS_O + (tile3 * 32 + 8 * q3) * 1024 + (j >> 2) * 4096);
        }
        SBAR();
        static_for<6>([&](auto IC_) {
            constexpr int i = decltype(IC_)::value, j = 6 * sb + i;
            const u32x4 a = j < 28 ? af[j < 28 ? j : 0] : ao[j >= 28 ? j - 28 : 0];
            acc0 = mfma16(w0[i % 3], a, acc0);
            if constexpr (sb + 2 < 6) p3_dma<(sb + 2 < 6 ? sb + 2 : 0), i>(W3);
            acc1 = mfma16(w1[i % 3], a, acc1);
            if (i + 3 < 6) { w0[i % 3] = tr_frag<1024>(wp0 + (i + 3) * 2048); w1[i % 3] = tr_frag<1024>(wp1 + (i + 3) * 2048); }
            SBAR();
        });
        if (sb == 2 && w == 0) poll_flags(P.ws + WS_FO + ((tb3 >> 1) * 16 + (lane & 15)) * 64, lane < 16, tag);
        if (sb < 4) asm volatile("s_waitcnt vmcnt(6) lgkmcnt(0)\n\ts_barrier" ::: "memory");
        else if (sb == 4) VM0_BAR();
        if (sb == 0 && tid == 0) st_flag(P.ws + WS_FO + (qb * 16 + head) * 64, tag);
        if (sb == 1) asm volatile("" ::"v"(tch0), "v"(tch1));
        if (sb == 2) STAMP(11);
        if (sb == 4) STAMP(12);
    });
    BAR();
    STAMP(13);
    LAS float* rx = (LAS float*)lds;
    float v[8];
    auto scatter = [&](auto QC_) {
        constexpr int Q = decltype(QC_)::value;
#pragma unroll
        for (int d = 0; d < 4; ++d) {
            if (d != Q) {
#pragma unroll
                for (int i = 0; i < 8; ++i) rx[(((tt3 * 4 + d) * 4 + Q) * 8 + i) * 64 + lane] = (d >> 1) ? acc1[8 * (d & 1) + i] : acc0[8 * (d & 1) + i];
            } else {
#pragma unroll
                for (int i = 0; i < 8; ++i) v[i] = (d >> 1) ? acc1[8 * (d & 1) + i] : acc0[8 * (d & 1) + i];
            }
        }
    };
    if (q3 == 0) scatter(IC<0>{}); else if (q3 == 1) scatter(IC<1>{}); else if (q3 == 2) scatter(IC<2>{}); else scatter(IC<3>{});
    BAR();
#pragma unroll
    for (int s = 1; s < 4; ++s) {
        const unsigned sq = (q3 + s) & 3;
#pragma unroll
        for (int i = 0; i < 8; ++i) v[i] += rx[(((tt3 * 4 + q3) * 4 + sq) * 8 + i) * 64 + lane];
    }
    u32x2 pk[2];
#pragma unroll
    for (int gq = 0; gq < 2; ++gq) {
        const float y0 = v[4 * gq] + __uint_as_float(xr[gq][0] << 16) + __uint_as_float(br[gq][0] << 16), y1 = v[4 * gq + 1] + __uint_as_float(xr[gq][0] & 0xffff0000u) + __uint_as_float(br[gq][0] & 0xffff0000u);
        const float y2 = v[4 * gq + 2] + __uint_as_float(xr[gq][1] << 16) + __uint_as_float(br[gq][1] << 16), y3 = v[4 * gq + 3] + __uint_as_float(xr[gq][1] & 0xffff0000u) + __uint_as_float(br[gq][1] & 0xffff0000u);
        pk[gq] = u32x2{cvtpk(y0, y1), cvtpk(y2, y3)};
    }
    {
        auto s0 = __builtin_amdgcn_permlane32_swap(pk[0][0], pk[1][0], false, false), s1 = __builtin_amdgcn_permlane32_swap(pk[0][1], pk[1][1], false, false);
        const u32x4 o4 = {s0[0], s1[0], s0[1], s1[1]};
        __builtin_amdgcn_raw_buffer_store_b128(o4, mk_rsrc(P.out, SEQ * DM * 2), (tok * DM + n0 + 8 * h) * 2, 0, OUT_AUX);
    }
    STAMP(14);
}

__global__ __launch_bounds__(512, 2) void fused_kernel(const void* x, const void* wqkv, const void* wo, const void* w1, const void* b1, const void* w2, const void* b2, void* out, char* ws) {
    extern __shared__ __attribute__((aligned(16))) char lds_raw[];
    LAS char* lds = (LAS char*)lds_raw;
    const Ptrs P{x, wqkv, wo, w1, b1, w2, b2, out, ws};
    STAMP(0);
    u32x4 qf0, qf1; float kn2; P3W W3;
    const u64 tag = phase1(P, lds, blockIdx.x, qf0, qf1, kn2, W3);
    phase23(P, lds, blockIdx.x, tag, qf0, qf1, kn2, W3);
}

extern "C" void kernel_launch(void* const* d_in, const int* in_sizes, int n_in, void* d_out, int out_size, void* d_ws, size_t ws_size, hipStream_t stream) {
    (void)in_sizes; (void)n_in; (void)out_size; (void)ws_size;
    (void)hipFuncSetAttribute((const void*)fused_kernel, hipFuncAttributeMaxDynamicSharedMemorySize, LDS_BYTES);
    fused_kernel<<<256, 512, LDS_BYTES, stream>>>(d_in[0], d_in[1], d_in[2], d_in[4], d_in[5], d_in[6], d_in[7], d_out, (char*)d_ws);
}
```

```cpp
#include <hip/hip_runtime.h>
#include <hip/hip_bf16.h>
#include <stdint.h>
#include <type_traits>
#include <utility>

#define DI __device__ __forceinline__
#define LAS __attribute__((address_space(3)))
#define GAS __attribute__((address_space(1)))
typedef float f32x16 __attribute__((ext_vector_type(16)));
typedef float f32x4 __attribute__((ext_vector_type(4)));
typedef float f32x2 __attribute__((ext_vector_type(2)));
typedef __bf16 bf16x8 __attribute__((ext_vector_type(8)));
typedef __bf16 bf16x2 __attribute__((ext_vector_type(2)));
typedef unsigned u32x4 __attribute__((ext_vector_type(4)));
typedef unsigned u32x2 __attribute__((ext_vector_type(2)));
typedef short s16x4 __attribute__((ext_vector_type(4)));
typedef unsigned long long u64;
typedef __amdgpu_buffer_rsrc_t rsrc_t;

constexpr int SEQ = 2048, DM = 448, NH = 16, DH = 28, FF = 1792;
constexpr unsigned WS_EPOCH = 0, WS_FQKV = 4096, WS_FH = WS_FQKV + 256 * 64, WS_FO = WS_FH + 256 * 64;
constexpr unsigned WS_KN2 = 512u << 10;
constexpr unsigned WS_Q = 1u << 20, WS_K = 3u << 20, WS_V = 5u << 20, WS_O = 7u << 20, WS_H = 9u << 20;
constexpr unsigned WS_BYTES = 17u << 20;
constexpr float C2 = 0.27264234f;
constexpr float LN_EPS = 1e-5f;
constexpr int AUX_SC1 = 16;
constexpr unsigned LDS_BYTES = 160 * 1024;
constexpr unsigned SPIN_MAX = 200000;
constexpr unsigned OOB = 0x7ffffff0u;
#define SAFE_BOUND 40.f
#define QD 1
#define QB 1
#define KQC 1
#define QX 1
#define QPF 3
#define PUBS 18
#define OUT_AUX 0
#define XL 1
#define XB 1
#define XA 1

DI rsrc_t mk_rsrc(const void* p, unsigned bytes) { return __builtin_amdgcn_make_buffer_rsrc((void*)p, (short)0, (int)bytes, 0x00020000); }
DI unsigned rfl(unsigned x) { return __builtin_amdgcn_readfirstlane(x); }
DI unsigned rfl_pad(unsigned x) { unsigned r = rfl(x); asm volatile("s_nop 4" : "+s"(r)); return r; }
DI void dma16(rsrc_t rs, unsigned voff, unsigned soff, unsigned ldsaddr) {
    asm volatile("s_mov_b32 m0, %0\n\ts_nop 0\n\tbuffer_load_dwordx4 %1, %2, %3 offen lds" ::"s"(ldsaddr), "v"(voff), "s"(rs), "s"(soff) : "memory");
}
DI void dma16_sc1(rsrc_t rs, unsigned voff, unsigned soff, unsigned ldsaddr) {
    asm volatile("s_mov_b32 m0, %0\n\ts_nop 0\n\tbuffer_load_dwordx4 %1, %2, %3 offen sc1 lds" ::"s"(ldsaddr), "v"(voff), "s"(rs), "s"(soff) : "memory");
}
#define WAIT_VM0() asm volatile("s_waitcnt vmcnt(0)" ::: "memory")
#define BAR() asm volatile("s_waitcnt lgkmcnt(0)\n\ts_barrier" ::: "memory")
#define VM0_BAR() asm volatile("s_waitcnt vmcnt(0) lgkmcnt(0)\n\ts_barrier" ::: "memory")
#define VMCNT(n) asm volatile("s_waitcnt vmcnt(%0)" ::"n"(n) : "memory")
#define VMCNT_BAR(n) asm volatile("s_waitcnt vmcnt(%0) lgkmcnt(0)\n\ts_barrier" ::"n"(n) : "memory")
#define SBAR() __builtin_amdgcn_sched_barrier(0)
#define PIN(x) asm volatile("" : "+v"(x))

template <int V> using IC = std::integral_constant<int, V>;
template <int... Is, class F> DI void static_for_(std::integer_sequence<int, Is...>, F&& f) { (f(IC<Is>{}), ...); }
template <int N, class F> DI void static_for(F&& f) { static_for_(std::make_integer_sequence<int, N>{}, f); }

DI f32x16 mfma16(u32x4 a, u32x4 b, f32x16 c) { return __builtin_amdgcn_mfma_f32_32x32x16_bf16(__builtin_bit_cast(bf16x8, a), __builtin_bit_cast(bf16x8, b), c, 0, 0, 0); }
DI f32x16 mfma_f32(float a, float b, f32x16 c) { return __builtin_amdgcn_mfma_f32_32x32x2f32(a, b, c, 0, 0, 0); }
DI unsigned cvtpk(float lo, float hi) { f32x2 v = {lo, hi}; return __builtin_bit_cast(unsigned, __builtin_convertvector(v, bf16x2)); }
DI float dot2(unsigned a, unsigned b, float c) { return __builtin_amdgcn_fdot2_f32_bf16(__builtin_bit_cast(bf16x2, a), __builtin_bit_cast(bf16x2, b), c, false); }
DI float swap_add(float x) { auto r = __builtin_amdgcn_permlane32_swap(__float_as_uint(x), __float_as_uint(x), false, false); return __uint_as_float(r[0]) + __uint_as_float(r[1]); }
DI float swap_max(float x) { auto r = __builtin_amdgcn_permlane32_swap(__float_as_uint(x), __float_as_uint(x), false, false); return fmaxf(__uint_as_float(r[0]), __uint_as_float(r[1])); }
template <int CTRL> DI float dpp_max(float x) { return fmaxf(x, __uint_as_float(__builtin_amdgcn_update_dpp(0u, __float_as_uint(x), CTRL, 0xf, 0xf, true))); }
DI float max32(float x) {
    x = dpp_max<0x128>(x); x = dpp_max<0x124>(x); x = dpp_max<0x122>(x); x = dpp_max<0x121>(x);
    auto sw = __builtin_amdgcn_permlane16_swap(__float_as_uint(x), __float_as_uint(x), false, false);
    return fmaxf(__uint_as_float(sw[0]), __uint_as_float(sw[1]));
}
template <int D2> DI u32x4 tr_frag(LAS char* p) {
    s16x4 lo = __builtin_amdgcn_ds_read_tr16_b64_v4i16((LAS s16x4*)p), hi = __builtin_amdgcn_ds_read_tr16_b64_v4i16((LAS s16x4*)(p + D2));
    u32x2 a = __builtin_bit_cast(u32x2, lo), b = __builtin_bit_cast(u32x2, hi);
    return u32x4{a[0], a[1], b[0], b[1]};
}
DI u32x4 pack8(const f32x16& v, int s) { return u32x4{cvtpk(v[8 * s], v[8 * s + 1]), cvtpk(v[8 * s + 2], v[8 * s + 3]), cvtpk(v[8 * s + 4], v[8 * s + 5]), cvtpk(v[8 * s + 6], v[8 * s + 7])}; }
DI void st16(rsrc_t rs, unsigned off, u32x4 v) { __builtin_amdgcn_raw_buffer_store_b128(v, rs, off, 0, AUX_SC1); }
DI u32x4 ld16(rsrc_t rs, unsigned voff, unsigned soff) { return __builtin_amdgcn_raw_buffer_load_b128(rs, voff, soff, AUX_SC1); }
DI float bf2f(unsigned short b) { return __uint_as_float((unsigned)b << 16); }
DI u64 ld_flag(const char* p) { return __hip_atomic_load((GAS const u64*)p, __ATOMIC_RELAXED, __HIP_MEMORY_SCOPE_AGENT); }
DI void st_flag(char* p, u64 v) { __hip_atomic_store((GAS u64*)p, v, __ATOMIC_RELAXED, __HIP_MEMORY_SCOPE_AGENT); }
DI void poll_flags(const char* p, bool use, u64 tag) {
    for (unsigned n = 0; n < SPIN_MAX; ++n) {
        const u64 v = use ? ld_flag(p) : tag;
        if (__all(v == tag)) break;
        __builtin_amdgcn_s_sleep(1);
    }
    asm volatile("" ::: "memory");
}
template <class T> DI T* opaque(T* p) { asm volatile("" : "+v"(p)); return p; }
#define LDF(p) (*(LAS u32x4*)(p))

#define STAMP(i) do {} while (0)
#define FSTAMP(i) do {} while (0)
struct Ptrs { const void* x; const void* wqkv; const void* wo; const void* w1; const void* b1; const void* w2; const void* b2; void* out; char* ws; };

constexpr unsigned L_STAT = 155648, L_CS = 156672;
DI void sum8(const u32x4& f, float& a, float& b) {
    float t0, t1, t2, t3;
    asm volatile("v_lshlrev_b32 %2, 16, %6\n\tv_and_b32 %3, 0xffff0000, %6\n\tv_lshlrev_b32 %4, 16, %7\n\tv_and_b32 %5, 0xffff0000, %7\n\t"
                 "v_add_f32 %0, %0, %2\n\tv_add_f32 %1, %1, %3\n\tv_add_f32 %0, %0, %4\n\tv_add_f32 %1, %1, %5\n\t"
                 "v_lshlrev_b32 %2, 16, %8\n\tv_and_b32 %3, 0xffff0000, %8\n\tv_lshlrev_b32 %4, 16, %9\n\tv_and_b32 %5, 0xffff0000, %9\n\t"
                 "v_add_f32 %0, %0, %2\n\tv_add_f32 %1, %1, %3\n\tv_add_f32 %0, %0, %4\n\tv_add_f32 %1, %1, %5"
                 : "+v"(a), "+v"(b), "=&v"(t0), "=&v"(t1), "=&v"(t2), "=&v"(t3) : "v"(f[0]), "v"(f[1]), "v"(f[2]), "v"(f[3]));
}
DI void gelu_a(float a0, float a1, float rstd, float c0, float& x0, float& x1, float& e0, float& e1) {
    asm volatile("v_mul_f32 %0, %4, %6\n\tv_mul_f32 %1, %5, %6\n\tv_mul_f32 %2, %0, %0\n\tv_mul_f32 %3, %1, %1\n\t"
                 "v_fmamk_f32 %2, %2, 0x3dd2d3e8, %7\n\tv_fmamk_f32 %3, %3, 0x3dd2d3e8, %7\n\tv_mul_f32 %2, %0, %2\n\tv_mul_f32 %3, %1, %3\n\t"
                 "v_exp_f32 %2, -%2\n\tv_exp_f32 %3, -%3"
                 : "=&v"(x0), "=&v"(x1), "=&v"(e0), "=&v"(e1) : "v"(a0), "v"(a1), "v"(rstd), "v"(c0));
}
DI unsigned gelu_b(float x0, float x1, float e0, float e1) {
    unsigned r;
    asm volatile("v_add_f32 %1, 1.0, %1\n\tv_add_f32 %2, 1.0, %2\n\tv_rcp_f32 %1, %1\n\tv_rcp_f32 %2, %2\n\ts_nop 0\n\tv_mul_f32 %1, %3, %1\n\tv_mul_f32 %2, %4, %2\n\tv_cvt_pk_bf16_f32 %0, %1, %2"
                 : "=v"(r), "+v"(e0), "+v"(e1) : "v"(x0), "v"(x1));
    return r;
}
#define MFMA_F32_PAD(x) asm volatile("s_nop 15\n\ts_nop 3" : "+v"(x))
DI float fmul_s(float a, float b) { float r; asm("v_mul_f32 %0, %1, %2" : "=v"(r) : "v"(a), "v"(b)); return r; }
DI float ffma_s(float a, float b, float c) { float r; asm("v_fma_f32 %0, %1, %2, %3" : "=v"(r) : "v"(a), "v"(b), "v"(c)); return r; }
DI float fadd_s(float a, float b) { float r; asm("v_add_f32 %0, %1, %2" : "=v"(r) : "v"(a), "v"(b)); return r; }
struct P1Dma { rsrc_t rqkv, rw1, rw2, rwo; unsigned vq0, vq1, vq2, vf, qsoff, qdst, fsoff, fdst, tsoff, tdst, usoff, udst, tdummy, vt2, vto; };

struct QkvAddr { LAS char* qa[4]; LAS char* qa2[4]; LAS char* xu[4]; };
template <int HALF, bool SWA, bool SWB>
DI void p1_qkv_pass(const Ptrs& P, const P1Dma& D, u32x4 (&xf)[28], const QkvAddr& A, bool is28, f32x16& accA, f32x16& accB, float& csA, float& csB) {
    constexpr int PF = 3;
    u32x4 wa[PF], wb[PF];
    float a0 = 0.f, a1 = 0.f, b0 = 0.f, b1 = 0.f;
    const unsigned ones = 0x3F803F80u;
    accA = f32x16{}; accB = f32x16{};
    auto rd = [&](auto SC) {
        constexpr int s = decltype(SC)::value; constexpr int o = s < 12 ? (s & ~3) * 3072 : ((s & ~3) - 12) * 3072;
        wa[s % PF] = tr_frag<768>((s < 12 ? A.qa[s & 3] : A.qa2[s & 3]) + o); if (HALF == 0) wb[s % PF] = tr_frag<768>((s < 12 ? A.qa[s & 3] : A.qa2[s & 3]) + o + 56);
    };
    FSTAMP(16);
    rd(IC<0>{}); rd(IC<1>{}); rd(IC<2>{});
    SBAR();
    static_for<28>([&](auto SC) {
        constexpr int s = decltype(SC)::value;
        if constexpr (s == 20) FSTAMP(25);
        u32x4 ca = wa[s % PF]; const u32x4 cb = wb[s % PF];
        if (HALF == 1) { ca[0] = is28 ? ones : ca[0]; ca[1] = is28 ? ones : ca[1]; ca[2] = is28 ? ones : ca[2]; ca[3] = is28 ? ones : ca[3]; }
        accA = SWA ? mfma16(xf[s], ca, accA) : mfma16(ca, xf[s], accA);
        if constexpr (!QD) {} else if constexpr (s < 6) dma16(D.rqkv, s % 3 == 0 ? D.vq0 : s % 3 == 1 ? D.vq1 : D.vq2, D.qsoff + (s / 3) * (16 * 2688), D.qdst + s * 1024);
        else if constexpr (s >= 8 && s < 16) dma16(D.rw1, D.vf, D.fsoff + (s - 8) * (4 * 3584), D.fdst + (s - 8) * 1024);
        else if constexpr (s >= 16 && s < 20) dma16(D.rw1, D.vf, D.tsoff + (s - 16) * (4 * 3584), D.tdst + (s - 16) * 1024);
        if (HALF == 0) accB = SWB ? mfma16(xf[s], cb, accB) : mfma16(cb, xf[s], accB);
        else accB = mfma16(xf[s], xf[s], accB);
        if constexpr (s + PF < 28 && !(s + PF >= 12 && s <= 11)) rd(IC<(s + PF < 28 ? s + PF : 0)>{});
        if constexpr (QX && s >= 4 && s < 8) {
#pragma unroll
            for (int i = 0; i < 3; ++i) { constexpr int dummy = 0; (void)dummy; const int j = 3 * (s - 4) + i; xf[16 + j] = LDF(A.xu[j & 3] + 128 * (j >> 2)); }
        }
        if constexpr (KQC && (s & 3) == 0) { sum8(ca, a0, a1); if (HALF == 0) sum8(cb, b0, b1); }
        SBAR();
        if constexpr (s == 12) {
            asm volatile("s_mov_b32 m0, %0\n\ts_nop 0\n\tbuffer_load_dword %1, %2, 0 offen lds\n\tbuffer_load_dword %3, %4, 0 offen lds" ::"s"(D.tdummy), "v"(D.vt2), "s"(D.rw2), "v"(D.vto), "s"(D.rwo) : "memory");
        }
        if constexpr (QB && s == 3) { VMCNT_BAR(6); STAMP(18); SBAR(); }
        if constexpr (QB && s == 7) { FSTAMP(19); BAR(); FSTAMP(20); STAMP(19); SBAR(); }
        if constexpr (s == 11) {
            FSTAMP(21); if (QB) VMCNT_BAR(4); FSTAMP(24); STAMP(20);
            rd(IC<12>{}); rd(IC<13>{}); rd(IC<14>{});
            SBAR();
        }
    });
    csA = a0 + a1; csB = b0 + b1;
}
DI void p1_ffn_pass(const Ptrs& P, LAS char* lds, const P1Dma& D, const u32x4 (&xf)[28], unsigned tt, unsigned off, f32x16& accA, f32x16& accB, float& csA, float& csB,
                    char* flag, u64 tag) {
    LAS char* q[4]; LAS char* q2[4]; LAS char* p[4]; LAS char* p2[4];
#pragma unroll
    for (int u = 0; u < 4; ++u) {
        const unsigned ro = off + ((u + tt) & 3) * 4096;
        q[u] = opaque(lds + 49152 + ro); q2[u] = opaque(lds + 114688 + ro); p[u] = opaque(lds + 49152 + (ro ^ 64)); p2[u] = opaque(lds + 114688 + (ro ^ 64));
    }
    constexpr int PF = 3;
    u32x4 wa[PF], wb[PF];
    float a0 = 0.f, a1 = 0.f, b0 = 0.f, b1 = 0.f;
    accA = f32x16{}; accB = f32x16{};
    auto rd = [&](auto SC) {
        constexpr int s = decltype(SC)::value;
        if constexpr (s < 16) { wa[s % PF] = tr_frag<1024>(q[s & 3] + (s & ~3) * 4096); wb[s % PF] = tr_frag<1024>(p[s & 3] + (s & ~3) * 4096); }
        else if constexpr (s < 24) { wa[s % PF] = tr_frag<1024>(q2[s & 3] + ((s & ~3) - 16) * 4096); wb[s % PF] = tr_frag<1024>(p2[s & 3] + ((s & ~3) - 16) * 4096); }
        else { wa[s % PF] = tr_frag<1024>(q[s & 3] - 49152); wb[s % PF] = tr_frag<1024>(p[s & 3] - 49152); }
    };
    rd(IC<0>{}); rd(IC<1>{}); rd(IC<2>{});
    SBAR();
    static_for<28>([&](auto SC) {
        constexpr int s = decltype(SC)::value;
        const u32x4 ca = wa[s % PF], cb = wb[s % PF];
        accA = mfma16(ca, xf[s], accA);
        accB = mfma16(cb, xf[s], accB);
        if constexpr (s + PF < 28) rd(IC<(s + PF < 28 ? s + PF : 0)>{});
        if constexpr ((s & 3) == 0) sum8(ca, a0, a1);
        if constexpr ((s & 3) == 0) sum8(cb, b0, b1);
        SBAR();
        if constexpr (s == 12) {
            VMCNT_BAR(8);
            STAMP(21);
            SBAR();
        }
        if constexpr (s == PUBS) {
            VMCNT_BAR(0);
            if (threadIdx.x == 0) st_flag(flag, tag);
            SBAR();
        }
    });
    csA = a0 + a1; csB = b0 + b1;
}
DI float p1_cs(LAS float* csb, unsigned l31) {
    float v[8];
#pragma unroll
    for (int i = 0; i < 8; ++i) v[i] = csb[i * 32 + l31];
    return ((v[0] + v[1]) + (v[2] + v[3])) + ((v[4] + v[5]) + (v[6] + v[7]));
}
struct P1Ctx { rsrc_t rws; char* ws; unsigned lane, l31, h, tile; float mu, sig, rstd; };
DI void p1_gelu(const P1Ctx& C, f32x16 acc, float cs, float bias, u32x4& pk0, u32x4& pk1) {
    acc = mfma_f32(C.h ? bias : cs, C.h ? C.sig : -C.mu, acc);
#pragma unroll
    for (int i = 0; i < 16; i += 2) {
        const f32x2 xv = f32x2{acc[i], acc[i + 1]} * C.rstd, t2 = xv * xv;
        const f32x2 pl = __builtin_elementwise_fma(t2, f32x2{0.1029432397f, 0.1029432397f}, f32x2{2.302208198f, 2.302208198f}), z = xv * pl;
        const f32x2 d = f32x2{__builtin_amdgcn_exp2f(-z[0]), __builtin_amdgcn_exp2f(-z[1])} + 1.f;
        const f32x2 y = xv * f32x2{__builtin_amdgcn_rcpf(d[0]), __builtin_amdgcn_rcpf(d[1])};
        acc[i] = y[0]; acc[i + 1] = y[1];
    }
    pk0 = pack8(acc, 0); pk1 = pack8(acc, 1);
}
DI void p1_store_h(const P1Ctx& C, unsigned ft, u32x4 pk0, u32x4 pk1) {
    const unsigned off = WS_H + (C.tile * 112 + 2 * ft) * 1024 + C.lane * 16;
    st16(C.rws, off, pk0); st16(C.rws, off + 1024, pk1);
}
DI void p1_epi_qkv(const P1Ctx& C, f32x16 acc, float cs, unsigned type, unsigned head) {
    const unsigned h = C.h, l31 = C.l31;
    const bool isv = type == 2;
    const float m0 = h ? 0.f : -C.mu, c0 = h ? 0.f : cs, r0 = h ? 0.f : (type == 0 ? C.rstd * C2 : C.rstd), k0 = (h || l31 >= 28) ? 0.f : 1.f;
    acc = mfma_f32(isv ? m0 : c0, isv ? c0 : m0, acc);
    f32x16 R = {}; R = mfma_f32(isv ? r0 : k0, isv ? k0 : r0, R);
    const float pv = (isv && l31 == 28) ? 1.f : 0.f, p12 = isv ? pv : ((type == 1 && h) ? 1.f : 0.f);
    f32x2 n2 = {0.f, 0.f};
#pragma unroll
    for (int i = 0; i < 16; i += 2) {
        const f32x2 t = __builtin_elementwise_fma(f32x2{acc[i], acc[i + 1]}, f32x2{R[i], R[i + 1]}, f32x2{i == 12 ? p12 : pv, pv});
        acc[i] = t[0]; acc[i + 1] = t[1];
        n2 = __builtin_elementwise_fma(t, t, n2);
    }
    const unsigned off = WS_Q + type * (2u << 20) + (head * 64 + C.tile) * 2048 + C.lane * 16;
    st16(C.rws, off, pack8(acc, 0)); st16(C.rws, off + 1024, pack8(acc, 1));
    const float kn = max32(swap_add(n2[0] + n2[1]));
    __builtin_amdgcn_raw_buffer_store_b32(__float_as_uint(kn), C.rws, (type == 1 && C.lane == 0) ? WS_KN2 + (head * 64 + C.tile) * 4 : OOB, 0, AUX_SC1);
}

DI u64 make_tag(u64 e) {
    e = ((u64)rfl((unsigned)(e >> 32)) << 32) | rfl((unsigned)e);
    u64 tag = (e + 0x9E3779B97F4A7C15ull); tag = (tag ^ (tag >> 30)) * 0xBF58476D1CE4E5B9ull; tag = (tag ^ (tag >> 27)) * 0x94D049BB133111EBull; tag ^= tag >> 31;
    return tag;
}
constexpr unsigned p3_region(int sb) { return (sb % 3) * 49152u; }
constexpr unsigned RING = 98304;
constexpr unsigned ring_off(int c) { return c % 4 == 2 ? 0u : c % 4 == 0 ? 16384u : c % 4 == 1 ? 32768u : 49152u; }
struct P3W { rsrc_t rw2, rwo; unsigned vw, vo0, vo1, soff, dst, hd0, vt2, vto; };
template <int SB, int I> DI void p3_dma(const P3W& W) {
    constexpr int j = 6 * SB + I;
    if constexpr (j < 28) dma16(W.rw2, W.vw, W.soff + j * (16 * DM * 2), W.dst + p3_region(SB) + I * 2048);
    else {
        dma16(W.rwo, ((j - 28) & 1) ? W.vo1 : W.vo0, W.hd0 + ((j - 28) >> 1) * (28 * DM * 2), W.dst + p3_region(SB) + I * 2048);
    }
}
DI P3W make_p3w(const Ptrs& P, unsigned lds0, int b, unsigned w, unsigned lane) {
    const unsigned r = b >> 3, cg3 = r < 28 ? r >> 2 : 0;
    P3W W3;
    const unsigned vcol = cg3 * 128 + (((lane >> 2) & 1) ^ ((lane >> 4) & 1)) * 64 + (lane & 3) * 16, rw = (w & 1) * 8 + (lane >> 3);
    W3.rw2 = mk_rsrc(P.w2, FF * DM * 2); W3.rwo = mk_rsrc(P.wo, DM * DM * 2);
    W3.vw = rw * (DM * 2) + vcol; W3.vo0 = W3.vw; W3.vo1 = W3.vw + 16 * (DM * 2);
    W3.soff = rfl_pad(28 * (w >> 1) * (16 * DM * 2)); W3.hd0 = rfl_pad(4 * (w >> 1) * (28 * DM * 2));
    W3.dst = rfl_pad(lds0 + (w >> 1) * 6 * 2048 + (w & 1) * 1024);
    {
        const unsigned L = w * 64 + lane, j = 12 + (L >> 4), row = L & 15, qq = r & 3;
        W3.vt2 = w < 4 ? ((28 * qq + j) * 16 + row) * (DM * 2) + cg3 * 128 : OOB;
        W3.vto = (w == 4 || w == 5) ? (28 * (4 * qq + ((j - 28) >> 1)) + 16 * ((j - 28) & 1) + row) * (DM * 2) + cg3 * 128 : OOB;
    }
    return W3;
}
DI void kv_first(const Ptrs& P, LAS char* lds, int b, unsigned w, unsigned lane, u32x4& qf0, u32x4& qf1, float& kn2) {
    const unsigned g = b & 7, r = b >> 3, head = 2 * g + (r >> 4), tile = (r & 15) * 4 + (w & 3);
    const rsrc_t rws = mk_rsrc(P.ws, WS_BYTES);
    const unsigned src = rfl_pad(((w & 1) ? WS_V : WS_K) + (head * 64 + (w >> 2) * 32 + ((w >> 1) & 1)) * 2048), wdst = rfl_pad((unsigned)(size_t)lds + 98304 + w * 2048);
    dma16_sc1(rws, lane * 16, src, wdst + 16384); dma16_sc1(rws, lane * 16 + 1024, src, wdst + 16384 + 1024);
    dma16_sc1(rws, lane * 16, src + 4096, wdst + 32768); dma16_sc1(rws, lane * 16 + 1024, src + 4096, wdst + 32768 + 1024);
    const unsigned qs = rfl_pad(WS_Q + (head * 64 + tile) * 2048), ks = rfl_pad(WS_KN2 + head * 256);
    asm volatile("buffer_load_dwordx4 %0, %3, %5, %6 offen sc1\n\tbuffer_load_dwordx4 %1, %3, %5, %6 offen offset:1024 sc1\n\tbuffer_load_dword %2, %4, %5, %7 offen sc1"
                 : "=&v"(qf0), "=&v"(qf1), "=&v"(kn2) : "v"(lane * 16), "v"(lane * 4), "s"(rws), "s"(qs), "s"(ks) : "memory");
}
DI u64 phase1(const Ptrs& P, LAS char* lds, int b, u32x4& qf0, u32x4& qf1, float& kn2, P3W& W3) {
    const unsigned tid = threadIdx.x, lane = tid & 63, l31 = lane & 31, h = lane >> 5;
    const unsigned w = rfl(tid >> 6), tt = w & 3, half = w >> 2;
    const unsigned g = b & 7, r = b >> 3, tb = 4 * (g >> 1) + (r & 3), cg = 8 * (g & 1) + (r >> 2);
    const unsigned lds0 = (unsigned)(size_t)lds;
    const rsrc_t rx = mk_rsrc(P.x, SEQ * DM * 2);
    P1Dma D;
    D.rqkv = mk_rsrc(P.wqkv, DM * 3 * DM * 2);
    const unsigned cb = 168 * cg, cb16 = cb & ~15u, dl = cb & 15u;
    FSTAMP(13);
    {
        const unsigned rowa = 2 * w + h;
        const unsigned va = rowa * 896 + ((l31 ^ (rowa & 15)) << 4), sa = tb * 128 * 896;
#pragma unroll
        for (unsigned i = 0; i < 8; ++i) dma16(rx, va, sa + i * (16 * 896), lds0 + (w + 8 * i) * 1024);
    }
    {
        unsigned long long pc; unsigned len;
        asm volatile("s_getpc_b64 %0\n\tfk_code_here:\n\ts_mov_b32 %1, fk_code_end-fk_code_here" : "=s"(pc), "=s"(len));
        const unsigned per = (len / 128 + 31) / 32, line = r * per + w * 64 + lane;
        const bool on = w * 64 + lane < per && line * 128 < len;
        const unsigned long long a = pc + (on ? line * 128 : 0);
        asm volatile("s_mov_b32 m0, %0\n\ts_nop 0\n\tglobal_load_lds_dword %1, off" ::"s"(lds0 + 151552 + w * 256), "v"(a) : "memory");
    }
    {
        auto vq = [&](unsigned p) { const unsigned row = p / 12, ch = p - row * 12; return ch < 11 ? row * 2688 + cb16 + ch * 16 : OOB; };
        D.vq0 = vq(lane); D.vq1 = vq(64 + lane); D.vq2 = vq(128 + lane);
    }
    const unsigned ntile = ((cg ^ (cg >> 1)) & 1) ? 3 : 4, fstart = 7 * (cg >> 1) + ((cg & 1) ? (((cg >> 1) & 1) ? 3 : 4) : 0);
    D.rw1 = mk_rsrc(P.w1, DM * FF * 2);
    { const unsigned rq = lane >> 4, sl = ((lane >> 2) & 3) ^ rq; D.vf = sl < ntile ? rq * 3584 + (fstart + sl) * 64 + (lane & 3) * 16 : OOB; }
    D.qsoff = (12 + 2 * w) * (16 * 2688); D.qdst = lds0 + 2 * w * 3072;
    D.fsoff = 2 * w * (16 * 3584); D.fdst = lds0 + 49152 + 2 * w * 4096;
    D.tsoff = (256 + 16 * w) * 3584; D.tdst = lds0 + 114688 + w * 4096;
    D.usoff = (384 + 8 * w) * 3584; D.udst = lds0 + 2 * w * 1024;
    D.rw2 = mk_rsrc(P.w2, FF * DM * 2); D.rwo = mk_rsrc(P.wo, DM * DM * 2); D.tdummy = lds0 + 151552 + w * 256;
    { const unsigned i = w * 8 + (lane & 7); D.vt2 = (lane < 8 && i < 49) ? (b * 49 + i) * 128 : OOB; D.vto = (lane < 8 && i < 13) ? (b * 13 + i) * 128 : OOB; }
    const u64 eraw = ld_flag(P.ws + WS_EPOCH);
    const unsigned short b1a = ((const unsigned short*)P.b1)[(fstart + 2 * half) * 32 + l31];
    const unsigned short b1b = ((const unsigned short*)P.b1)[(2 * half + 1 < ntile ? fstart + 2 * half + 1 : fstart) * 32 + l31];
    {
        const unsigned so = w * (16 * 2688), ds = lds0 + 114688 + w * 3072;
        if (w < 4) { dma16(D.rqkv, D.vq0, so, ds); dma16(D.rqkv, D.vq1, so, ds + 1024); dma16(D.rqkv, D.vq2, so, ds + 2048); }
        dma16(D.rqkv, D.vq0, so + 4 * (16 * 2688), ds + 4 * 3072); dma16(D.rqkv, D.vq1, so + 4 * (16 * 2688), ds + 4 * 3072 + 1024); dma16(D.rqkv, D.vq2, so + 4 * (16 * 2688), ds + 4 * 3072 + 2048);
    }
#pragma unroll
    for (unsigned i = 0; i < 6; ++i) {
        const unsigned p = 64 * i + lane, rr = p / 24, c = p - rr * 24, row = 16 * w + rr;
        dma16(rx, row * 896 + 512 + ((c ^ ((row >> 1) & 7)) << 4), tb * 128 * 896, lds0 + 65536 + (6 * w + i) * 1024);
    }
    const rsrc_t rws = mk_rsrc(P.ws, WS_BYTES);
    LAS float* stat = (LAS float*)(lds + L_STAT);
    LAS float* csb = (LAS float*)(lds + L_CS);
    P1Ctx C{rws, P.ws, lane, l31, h, tb * 4 + tt, 0.f, 0.f, 0.f};
    const unsigned G = lane >> 4, q4 = (lane & 15) >> 2;
    const unsigned trq = (8 * (G >> 1) + q4) * 192 + dl + (G & 1) * 32 + (lane & 3) * 8 + half * 112;
    const unsigned trf = (8 * (G >> 1) + q4) * 256 + (G & 1) * 32 + (lane & 3) * 8;
    f32x16 accA, accB; float csA, csB;
    const unsigned tA = 3 * cg + 2 * half, tB = 3 * cg + 1, tyA = tA >> 4, tyB = tB >> 4;
    QkvAddr QA;
    {
        LAS char* xb0 = lds + 65536 + (tt * 32 + l31) * 384; const unsigned gswb = (l31 >> 1) & 7;
#pragma unroll
        for (int u = 0; u < 4; ++u) {
            const unsigned ro = ((u + tt) & 3) * 3072;
            QA.qa[u] = opaque(lds + 114688 + trq + ro); QA.qa2[u] = opaque(lds + trq + ro);
            QA.xu[u] = opaque(xb0 + (((2 * ((u + tt) & 3) + h) ^ gswb) << 4));
        }
    }
    const bool m28 = l31 == 28;
    {
        const unsigned i5 = w * 5 + lane, i7 = w * 7 + lane, g5 = b * 37 + i5;
        const unsigned v5 = (lane < 5 && i5 < 37 && g5 < 9408) ? g5 * 128 : OOB, v7 = (lane < 7 && i7 < 49) ? (b * 49 + i7) * 128 : OOB;
        asm volatile("s_mov_b32 m0, %0\n\ts_nop 0\n\tbuffer_load_dword %1, %2, 0 offen lds\n\tbuffer_load_dword %3, %4, 0 offen lds" ::"s"(D.tdummy), "v"(v5), "s"(D.rqkv), "v"(v7), "s"(D.rw1) : "memory");
    }
    FSTAMP(14);
    VMCNT_BAR(11);
    STAMP(1);
    u32x4 xf[28];
    {
        LAS char* xa = lds + (tt * 32 + l31) * 512;
#pragma unroll
        for (int u = 0; u < 4; ++u) {
            const unsigned c = 2 * ((u + tt) & 3) + h;
            LAS char* x0 = xa + ((c ^ (l31 & 15)) << 4); LAS char* x1 = xa + (((c + 8) ^ (l31 & 15)) << 4);
            xf[u] = LDF(x0); xf[4 + u] = LDF(x1); xf[8 + u] = LDF(x0 + 256); xf[12 + u] = LDF(x1 + 256);
        }
    }
    const u64 tag = make_tag(eraw);
    VMCNT_BAR(8);
    STAMP(2);
    if (half == 0) {
        if (tyA == 2) p1_qkv_pass<0, true, true>(P, D, xf, QA, m28, accA, accB, csA, csB);
        else if (tyB == 2) p1_qkv_pass<0, false, true>(P, D, xf, QA, m28, accA, accB, csA, csB);
        else p1_qkv_pass<0, false, false>(P, D, xf, QA, m28, accA, accB, csA, csB);
        csb[0 * 256 + tt * 64 + lane] = csA; csb[1 * 256 + tt * 64 + lane] = csB;
    } else {
        float a1;
        if (tyA == 2) {
            p1_qkv_pass<1, true, false>(P, D, xf, QA, m28, accA, accB, csA, csB);
            if (l31 == 28) {
#pragma unroll
                for (int i = 0; i < 16; ++i) stat[(tt * 32 + (i & 3) + 8 * (i >> 2) + 4 * h) * 2] = accA[i];
            }
            asm volatile("s_waitcnt lgkmcnt(0)" ::: "memory");
            a1 = stat[(tt * 32 + l31) * 2];
        } else {
            p1_qkv_pass<1, false, false>(P, D, xf, QA, m28, accA, accB, csA, csB);
            auto sw = __builtin_amdgcn_permlane32_swap(__float_as_uint(accA[12]), __float_as_uint(accA[12]), false, false);
            a1 = __uint_as_float(sw[1]);
        }
        csb[2 * 256 + tt * 64 + lane] = csA;
        const unsigned ri = (l31 & 3) + 4 * (l31 >> 3);
        float dg = 0.f;
#pragma unroll
        for (int i = 0; i < 16; ++i) dg = ri == (unsigned)i ? accB[i] : dg;
        const float a2 = swap_add(h == ((l31 >> 2) & 1) ? dg : 0.f);
        const float m = a1 * (1.f / DM), var = fmaxf(a2 * (1.f / DM) - m * m, 0.f) + LN_EPS;
        if (!h) *(LAS f32x2*)(stat + (tt * 32 + l31) * 2) = f32x2{m, __builtin_amdgcn_rsqf(var)};
    }
    FSTAMP(2);
    VMCNT_BAR(4);
    STAMP(3);
    dma16(D.rw1, D.vf, D.usoff, D.udst); dma16(D.rw1, D.vf, D.usoff + 4 * 3584, D.udst + 1024);
    { const f32x2 st = *(LAS f32x2*)(stat + (tt * 32 + l31) * 2); C.mu = st[0]; C.rstd = st[1]; C.sig = __builtin_amdgcn_rcpf(st[1]); }
    {
        const float c0 = p1_cs(csb + 2 * half * 256, l31), c1 = p1_cs(csb + 256, l31);
        p1_epi_qkv(C, accA, c0, tyA, tA & 15);
        if (half == 0) p1_epi_qkv(C, accB, c1, tyB, tB & 15);
        else asm volatile("buffer_store_dword %0, %0, %1, 0 offen sc1\n\tbuffer_store_dword %0, %0, %1, 0 offen sc1\n\tbuffer_store_dword %0, %0, %1, 0 offen sc1" ::"v"(OOB), "s"(rws) : "memory");
    }
    FSTAMP(5);
    STAMP(4);
    const unsigned head2 = 2 * g + (r >> 4), qb2 = r & 15;
    const char* qflag;
    { const unsigned cq = head2 / 3, ck = (16 + head2) / 3, cv = (32 + head2) / 3;
      const unsigned idx = lane < 16 ? ck * 16 + lane : lane < 32 ? cv * 16 + (lane - 16) : cq * 16 + qb2; qflag = P.ws + WS_FQKV + (lane <= 32 ? idx : 0) * 64; }
    p1_ffn_pass(P, lds, D, xf, tt, trf + 64 * ((2 * half) ^ q4), accA, accB, csA, csB, P.ws + WS_FQKV + (cg * 16 + tb) * 64, tag);
    csb[(3 + 2 * half) * 256 + tt * 64 + lane] = csA; csb[(4 + 2 * half) * 256 + tt * 64 + lane] = csB;
    W3 = make_p3w(P, lds0, b, w, lane);
    STAMP(22);
    BAR();
    STAMP(23);
    {
        const bool second = 2 * half + 1 < ntile;
        const float c0 = p1_cs(csb + (3 + 2 * half) * 256, l31), c1 = p1_cs(csb + (4 + 2 * half) * 256, l31);
        u32x4 pk0, pk1;
        FSTAMP(27);
        poll_flags(qflag, lane <= 32, tag);
        FSTAMP(28);
        kv_first(P, lds, b, w, lane, qf0, qf1, kn2);
        FSTAMP(29);
        p1_gelu(C, accA, c0, bf2f(b1a), pk0, pk1); p1_store_h(C, fstart + 2 * half, pk0, pk1);
        if (second) { p1_gelu(C, accB, c1, bf2f(b1b), pk0, pk1); p1_store_h(C, fstart + 2 * half + 1, pk0, pk1); }
        else { asm volatile("buffer_store_dword %0, %0, %1, 0 offen sc1\n\tbuffer_store_dword %0, %0, %1, 0 offen sc1" ::"v"(OOB), "s"(rws) : "memory"); }
    }
    FSTAMP(12);
    STAMP(6);
    return tag;
}

constexpr int att_aflo(int T) { return (!XA || T < 6) ? 0 : (T - 6) * 28 / 26; }
constexpr int att_kv(int T) { return (XL && (T & 1) == 0 && (T >> 1) + 3 < 16) ? 2 : 0; }
constexpr int att_w(int T) { return T < 12 ? 1 : T == 28 ? 2 : 0; }
constexpr int att_all(int T0, int T1) { int n = 0; for (int T = T0; T < T1; ++T) n += att_kv(T) + att_w(T) + (att_aflo(T + 1) - att_aflo(T)); return n; }
#define EXP4(S, i) do { S[i] = __builtin_amdgcn_exp2f(S[i]); S[i + 1] = __builtin_amdgcn_exp2f(S[i + 1]); S[i + 2] = __builtin_amdgcn_exp2f(S[i + 2]); S[i + 3] = __builtin_amdgcn_exp2f(S[i + 3]); } while (0)
template <int KH>
DI void att_fast(LAS char* lds, rsrc_t rws, const P3W& W3, unsigned lane, unsigned w, unsigned src, unsigned wdst, unsigned afsrc, u32x4 qf0, u32x4 qf1, f32x16& o, u32x4 (&af)[28], const char* hflag, char* myh, u64 tag, unsigned& tch0, unsigned& tch1) {
    LAS char* kvb = opaque(lds + RING + KH * 8192 + lane * 16);
    const unsigned vo = lane * 16, vo1 = lane * 16 + 1024;
    src = rfl_pad(src); wdst = rfl_pad(wdst); afsrc = rfl_pad(afsrc);
    const f32x16 zero = {};
    f32x16 sc[2];
    u32x4 k0 = LDF(kvb + ring_off(0)), k1 = LDF(kvb + ring_off(0) + 1024), v0 = LDF(kvb + ring_off(0) + 2048), v1 = LDF(kvb + ring_off(0) + 3072), p1 = {0u, 0u, 0u, 0u}, v1p = {0u, 0u, 0u, 0u};
    sc[0] = mfma16(k0, qf0, zero); sc[0] = mfma16(k1, qf1, sc[0]);
    k0 = LDF(kvb + ring_off(0) + 4096); k1 = LDF(kvb + ring_off(0) + 4096 + 1024);
    static_for<32>([&](auto TC) {
        constexpr int T = decltype(TC)::value, c = T >> 1;
        f32x16& S = sc[T & 1]; f32x16& SN = sc[(T + 1) & 1];
        constexpr int o1 = ring_off((T + 1) >> 1) + ((T + 1) & 1) * 4096, o2 = ring_off((T + 2) >> 1) + ((T + 2) & 1) * 4096;
        u32x4 p0, k0n, k1n, v0n, v1n;
        auto chunk_barrier = [&](auto OPS) {
            constexpr int younger = decltype(OPS)::value;
            if constexpr (T == 5 && KH == 0) { if (w == 0) { PIN(p1); PIN(o); poll_flags(hflag, lane < 16, tag); } }
            if (XB) asm volatile("s_waitcnt vmcnt(%0) lgkmcnt(%1)\n\ts_barrier" ::"n"(younger), "n"(KH == 0 ? 4 : 2) : "memory");
            if constexpr (T == 1 && KH == 0) { if (w == 0) { PIN(p1); PIN(o); if (lane == 0) st_flag(myh, tag); } }
        };
        S[0] = __builtin_amdgcn_exp2f(S[0]); S[1] = __builtin_amdgcn_exp2f(S[1]);
        if (T > 0) o = mfma16(v1p, p1, o);
        S[2] = __builtin_amdgcn_exp2f(S[2]); S[3] = __builtin_amdgcn_exp2f(S[3]);
        if (T + 2 < 32) { k0n = LDF(kvb + o2); k1n = LDF(kvb + o2 + 1024); }
        p0[0] = cvtpk(S[0], S[1]); p0[1] = cvtpk(S[2], S[3]); SBAR();
        if (T + 1 < 32) SN = mfma16(k0, qf0, zero);
        EXP4(S, 4);
        if constexpr (att_kv(T) != 0) { dma16_sc1(rws, vo, src + (c + 3) * 4096, wdst + ring_off(c + 3)); dma16_sc1(rws, vo1, src + (c + 3) * 4096, wdst + ring_off(c + 3) + 1024); }
        if constexpr (T < 12) p3_dma<T / 6, T % 6>(W3);
        if constexpr (T == 28) { tch0 = __builtin_amdgcn_raw_buffer_load_b32(W3.rw2, W3.vt2, 0, 0); tch1 = __builtin_amdgcn_raw_buffer_load_b32(W3.rwo, W3.vto, 0, 0); }
        static_for<att_aflo(T + 1) - att_aflo(T)>([&](auto JC) { constexpr int j = att_aflo(T) + decltype(JC)::value; af[j] = ld16(rws, vo + (j & 3) * 1024, afsrc + (j >> 2) * 4096); });
        p0[2] = cvtpk(S[4], S[5]); p0[3] = cvtpk(S[6], S[7]); SBAR();
        constexpr int younger = c >= 1 ? att_all(2 * c - 2, T + 1) - att_kv(2 * c - 2) : att_all(0, T + 1);
        if constexpr (KH == 1 && (T & 1) == 1 && T < 31) chunk_barrier(IC<younger>{});
        if (T + 1 < 32) SN = mfma16(k1, qf1, SN);
        EXP4(S, 8);
        if (T + 1 < 32) { v0n = LDF(kvb + o1 + 2048); v1n = LDF(kvb + o1 + 3072); }
        p1[0] = cvtpk(S[8], S[9]); p1[1] = cvtpk(S[10], S[11]); SBAR();
        o = mfma16(v0, p0, o);
        EXP4(S, 12);
        p1[2] = cvtpk(S[12], S[13]); p1[3] = cvtpk(S[14], S[15]); SBAR();
        v1p = v1;
        if (T + 2 < 32) { k0 = k0n; k1 = k1n; }
        if (T + 1 < 32) { v0 = v0n; v1 = v1n; }
        if constexpr (KH == 0 && (T & 1) == 1 && T < 31) chunk_barrier(IC<younger>{});
    });
    o = mfma16(v1p, p1, o);
}

DI void phase23(const Ptrs& P, LAS char* lds, int b, u64 tag, u32x4 qf0, u32x4 qf1, float kn2, const P3W& W3) {
    const unsigned tid = threadIdx.x, lane = tid & 63, l31 = lane & 31, h = lane >> 5;
    const unsigned w = rfl(tid >> 6), qt = w & 3, kh = w >> 2;
    const unsigned g = b & 7, r = b >> 3, head = 2 * g + (r >> 4), qb = r & 15, tile = qb * 4 + qt;
    const unsigned lds0 = (unsigned)(size_t)lds;
    const rsrc_t rws = mk_rsrc(P.ws, WS_BYTES);
    const bool p3 = r < 28;
    const unsigned tt3 = w & 1, q3 = w >> 1, tb3 = 4 * g + (r & 3), cg3 = p3 ? r >> 2 : 0, tile3 = tb3 * 2 + tt3;
    STAMP(7);
    const unsigned src = rfl_pad(((w & 1) ? WS_V : WS_K) + (head * 64 + kh * 32 + ((w >> 1) & 1)) * 2048);
    const unsigned wdst = rfl_pad(lds0 + RING + w * 2048);
#define STAGE(c, off) do { dma16_sc1(rws, lane * 16, src + (c) * 4096, wdst + (off)); dma16_sc1(rws, lane * 16 + 1024, src + (c) * 4096, wdst + (off) + 1024); } while (0)
    asm volatile("s_waitcnt vmcnt(4)" : "+v"(qf0), "+v"(qf1), "+v"(kn2)::"memory");
    kn2 = swap_max(max32(kn2));
    float qn2 = 0.f;
#pragma unroll
    for (int e = 0; e < 4; ++e) { qn2 = dot2(qf0[e], qf0[e], qn2); qn2 = dot2(qf1[e], qf1[e], qn2); }
    qn2 = swap_add(qn2);
    const float bound = __builtin_amdgcn_sqrtf(qn2 * kn2) * 1.01f + 0.01f;
    const bool safe = __all(bound <= SAFE_BOUND);
    float mrun;
    if (safe) {
        const unsigned nb = cvtpk(-bound, 0.f) & 0xffffu;
        if (h) qf1[2] = (qf1[2] & 0xffff0000u) | nb;
        mrun = __uint_as_float((nb & 0x7fffu) << 16);
    } else mrun = -INFINITY;
    PIN(qf1); PIN(mrun);
    STAGE(2, ring_off(2));
    BAR();
    char* myh = P.ws + WS_FH + ((4 * (g >> 1) + (r & 3)) * 16 + 8 * (g & 1) + (r >> 2)) * 64;
    STAMP(8);
    f32x16 o = {};
    u32x4 af[28];
    unsigned tch0 = 0, tch1 = 0;
    const unsigned afsrc = WS_H + (tile3 * 112 + 28 * q3) * 1024;
    const char* hflag = P.ws + WS_FH + ((tb3 >> 1) * 16 + (lane & 15)) * 64;
    if (safe) {
        if (kh == 0) att_fast<0>(lds, rws, W3, lane, w, src, wdst, afsrc, qf0, qf1, o, af, hflag, myh, tag, tch0, tch1);
        else att_fast<1>(lds, rws, W3, lane, w, src, wdst, afsrc, qf0, qf1, o, af, hflag, myh, tag, tch0, tch1);
    } else {
        const f32x16 zero = {};
        LAS char* kvb = lds + RING + kh * 8192 + lane * 16;
        static_for<12>([&](auto TC) { constexpr int T = decltype(TC)::value; p3_dma<T / 6, T % 6>(W3); });
#pragma unroll 1
        for (unsigned c = 0; c < 16; ++c) {
            auto roff = [](unsigned c) { return (c & 3) == 2 ? 0u : (c & 3) == 0 ? 16384u : (c & 3) == 1 ? 32768u : 49152u; };
            if (c + 3 < 16) STAGE(c + 3, roff(c + 3));
#pragma unroll 1
            for (int t = 0; t < 2; ++t) {
                LAS char* kv = kvb + roff(c) + t * 4096;
                const u32x4 k0 = LDF(kv), k1 = LDF(kv + 1024), v0 = LDF(kv + 2048), v1 = LDF(kv + 3072);
                f32x16 s = mfma16(k0, qf0, zero); s = mfma16(k1, qf1, s);
                float m = s[0];
#pragma unroll
                for (int i = 1; i < 16; ++i) m = fmaxf(m, s[i]);
                m = fmaxf(swap_max(m), mrun);
                const float f = __builtin_amdgcn_exp2f(mrun - m);
                mrun = m;
#pragma unroll
                for (int i = 0; i < 16; ++i) { s[i] = __builtin_amdgcn_exp2f(s[i] - m); o[i] *= f; }
                o = mfma16(v0, pack8(s, 0), o); o = mfma16(v1, pack8(s, 1), o);
            }
            if (c == 2 && w == 0) poll_flags(hflag, lane < 16, tag);
            if (c < 15) VM0_BAR();
            if (c == 0 && tid == 0) st_flag(myh, tag);
        }
#pragma unroll
        for (int j = 0; j < 28; ++j) af[j] = ld16(rws, lane * 16, afsrc + j * 1024);
    }
    BAR();
    STAMP(9);
    LAS float* ex = (LAS float*)(lds + 147456 + qt * 4096);
    if (kh == 1) {
        if (h) o[15] = mrun;
#pragma unroll
        for (int i = 0; i < 16; ++i) ex[i * 64 + lane] = o[i];
    }
    BAR();
    if (kh == 0) {
        const float m2 = ex[15 * 64 + 32 + l31], m = fmaxf(mrun, m2), f1 = __builtin_amdgcn_exp2f(mrun - m), f2 = __builtin_amdgcn_exp2f(m2 - m);
#pragma unroll
        for (int i = 0; i < 16; ++i) o[i] = o[i] * f1 + ex[i * 64 + lane] * f2;
        auto sw = __builtin_amdgcn_permlane32_swap(__float_as_uint(o[12]), __float_as_uint(o[12]), false, false);
        const float l = __uint_as_float(sw[1]);
        const float il = __builtin_amdgcn_rcpf(l);
#pragma unroll
        for (int i = 0; i < 16; ++i) o[i] *= il;
        if (h) { o[12] = 0.f; o[13] = 0.f; o[14] = 0.f; o[15] = 0.f; }
        const unsigned off = WS_O + (tile * 16 + head) * 2048 + lane * 16;
        st16(rws, off, pack8(o, 0)); st16(rws, off + 1024, pack8(o, 1));
    }
    STAMP(10);
    if (!p3) {
        VM0_BAR();
        if (tid == 0) st_flag(P.ws + WS_FO + (qb * 16 + head) * 64, tag);
        if (b == 255 && w == 0) {
#pragma unroll 1
            for (int i = 0; i < 4; ++i) poll_flags(P.ws + WS_FQKV + (i * 64 + lane) * 64, true, tag);
            if (lane == 0) st_flag(P.ws + WS_EPOCH, tag);
        }
        return;
    }
    const unsigned q4 = (lane & 15) >> 2, trb = (4 * (lane >> 5) + q4) * 128 + ((lane >> 4) & 1) * 32 + (lane & 3) * 8 + q3 * 12 * 1024;
    const unsigned tro0 = trb + ((q4 >> 1) & 1) * 64, tro1 = trb + (((q4 >> 1) & 1) ^ 1) * 64;
    const unsigned tok = tile3 * 32 + l31, n0 = cg3 * 64 + q3 * 16;
    u32x2 xr[2], br[2];
#pragma unroll
    for (int gq = 0; gq < 2; ++gq) {
        const unsigned n = n0 + 8 * gq + 4 * h;
        xr[gq] = *(const u32x2*)((const char*)P.x + (tok * DM + n) * 2); br[gq] = *(const u32x2*)((const char*)P.b2 + n * 2);
    }
    f32x16 acc0 = {}, acc1 = {};
    u32x4 ao[8];
    static_for<6>([&](auto SC) {
        constexpr int sb = decltype(SC)::value;
        LAS char* wp0 = opaque(lds + p3_region(sb) + tro0); LAS char* wp1 = opaque(lds + p3_region(sb) + tro1);
        u32x4 w0[3], w1[3];
#pragma unroll
        for (int i = 0; i < 3; ++i) { w0[i] = tr_frag<1024>(wp0 + i * 2048); w1[i] = tr_frag<1024>(wp1 + i * 2048); }
        if (sb == 3) {
#pragma unroll
            for (int j = 0; j < 8; ++j) ao[j] = ld16(rws, lane * 16 + (j & 3) * 1024, WS_O + (tile3 * 32 + 8 * q3) * 1024 + (j >> 2) * 4096);
        }
        SBAR();
        static_for<6>([&](auto IC_) {
            constexpr int i = decltype(IC_)::value, j = 6 * sb + i;
            const u32x4 a = j < 28 ? af[j < 28 ? j : 0] : ao[j >= 28 ? j - 28 : 0];
            acc0 = mfma16(w0[i % 3], a, acc0);
            if constexpr (sb + 2 < 6) p3_dma<(sb + 2 < 6 ? sb + 2 : 0), i>(W3);
            acc1 = mfma16(w1[i % 3], a, acc1);
            if (i + 3 < 6) { w0[i % 3] = tr_frag<1024>(wp0 + (i + 3) * 2048); w1[i % 3] = tr_frag<1024>(wp1 + (i + 3) * 2048); }
            SBAR();
        });
        if (sb == 2 && w == 0) poll_flags(P.ws + WS_FO + ((tb3 >> 1) * 16 + (lane & 15)) * 64, lane < 16, tag);
        if (sb < 4) asm volatile("s_waitcnt vmcnt(6) lgkmcnt(0)\n\ts_barrier" ::: "memory");
        else if (sb == 4) VM0_BAR();
        if (sb == 0 && tid == 0) st_flag(P.ws + WS_FO + (qb * 16 + head) * 64, tag);
        if (sb == 1) asm volatile("" ::"v"(tch0), "v"(tch1));
        if (sb == 2) STAMP(11);
        if (sb == 4) STAMP(12);
    });
    BAR();
    STAMP(13);
    LAS float* rx = (LAS float*)lds;
    float v[8];
    auto scatter = [&](auto QC_) {
        constexpr int Q = decltype(QC_)::value;
#pragma unroll
        for (int d = 0; d < 4; ++d) {
            if (d != Q) {
#pragma unroll
                for (int i = 0; i < 8; ++i) rx[(((tt3 * 4 + d) * 4 + Q) * 8 + i) * 64 + lane] = (d >> 1) ? acc1[8 * (d & 1) + i] : acc0[8 * (d & 1) + i];
            } else {
#pragma unroll
                for (int i = 0; i < 8; ++i) v[i] = (d >> 1) ? acc1[8 * (d & 1) + i] : acc0[8 * (d & 1) + i];
            }
        }
    };
    if (q3 == 0) scatter(IC<0>{}); else if (q3 == 1) scatter(IC<1>{}); else if (q3 == 2) scatter(IC<2>{}); else scatter(IC<3>{});
    BAR();
#pragma unroll
    for (int s = 1; s < 4; ++s) {
        const unsigned sq = (q3 + s) & 3;
#pragma unroll
        for (int i = 0; i < 8; ++i) v[i] += rx[(((tt3 * 4 + q3) * 4 + sq) * 8 + i) * 64 + lane];
    }
    u32x2 pk[2];
#pragma unroll
    for (int gq = 0; gq < 2; ++gq) {
        const float y0 = v[4 * gq] + __uint_as_float(xr[gq][0] << 16) + __uint_as_float(br[gq][0] << 16), y1 = v[4 * gq + 1] + __uint_as_float(xr[gq][0] & 0xffff0000u) + __uint_as_float(br[gq][0] & 0xffff0000u);
        const float y2 = v[4 * gq + 2] + __uint_as_float(xr[gq][1] << 16) + __uint_as_float(br[gq][1] << 16), y3 = v[4 * gq + 3] + __uint_as_float(xr[gq][1] & 0xffff0000u) + __uint_as_float(br[gq][1] & 0xffff0000u);
        pk[gq] = u32x2{cvtpk(y0, y1), cvtpk(y2, y3)};
    }
    {
        auto s0 = __builtin_amdgcn_permlane32_swap(pk[0][0], pk[1][0], false, false), s1 = __builtin_amdgcn_permlane32_swap(pk[0][1], pk[1][1], false, false);
        const u32x4 o4 = {s0[0], s1[0], s0[1], s1[1]};
        __builtin_amdgcn_raw_buffer_store_b128(o4, mk_rsrc(P.out, SEQ * DM * 2), (tok * DM + n0 + 8 * h) * 2, 0, OUT_AUX);
    }
    STAMP(14);
    asm volatile("fk_code_end:");
}

__global__ __launch_bounds__(512, 2) void fused_kernel(const void* x, const void* wqkv, const void* wo, const void* w1, const void* b1, const void* w2, const void* b2, void* out, char* ws) {
    extern __shared__ __attribute__((aligned(16))) char lds_raw[];
    LAS char* lds = (LAS char*)lds_raw;
    const Ptrs P{x, wqkv, wo, w1, b1, w2, b2, out, ws};
    STAMP(0);
    u32x4 qf0, qf1; float kn2; P3W W3;
    const u64 tag = phase1(P, lds, blockIdx.x, qf0, qf1, kn2, W3);
    phase23(P, lds, blockIdx.x, tag, qf0, qf1, kn2, W3);
}

extern "C" void kernel_launch(void* const* d_in, const int* in_sizes, int n_in, void* d_out, int out_size, void* d_ws, size_t ws_size, hipStream_t stream) {
    (void)in_sizes; (void)n_in; (void)out_size; (void)ws_size;
    (void)hipFuncSetAttribute((const void*)fused_kernel, hipFuncAttributeMaxDynamicSharedMemorySize, LDS_BYTES);
    fused_kernel<<<256, 512, LDS_BYTES, stream>>>(d_in[0], d_in[1], d_in[2], d_in[4], d_in[5], d_in[6], d_in[7], d_out, (char*)d_ws);
}
```

```cpp
#include <hip/hip_runtime.h>
#include <hip/hip_bf16.h>
#include <stdint.h>
#include <type_traits>
#include <utility>

#define DI __device__ __forceinline__
#define LAS __attribute__((address_space(3)))
#define GAS __attribute__((address_space(1)))
typedef float f32x16 __attribute__((ext_vector_type(16)));
typedef float f32x4 __attribute__((ext_vector_type(4)));
typedef float f32x2 __attribute__((ext_vector_type(2)));
typedef __bf16 bf16x8 __attribute__((ext_vector_type(8)));
typedef __bf16 bf16x2 __attribute__((ext_vector_type(2)));
typedef unsigned u32x4 __attribute__((ext_vector_type(4)));
typedef unsigned u32x2 __attribute__((ext_vector_type(2)));
typedef short s16x4 __attribute__((ext_vector_type(4)));
typedef unsigned long long u64;
typedef __amdgpu_buffer_rsrc_t rsrc_t;

constexpr int SEQ = 2048, DM = 448, NH = 16, DH = 28, FF = 1792;
constexpr unsigned WS_EPOCH = 0, WS_FQKV = 4096, WS_FH = WS_FQKV + 256 * 64, WS_FO = WS_FH + 256 * 64;
constexpr unsigned WS_KN2 = 512u << 10;
constexpr unsigned WS_Q = 1u << 20, WS_K = 3u << 20, WS_V = 5u << 20, WS_O = 7u << 20, WS_H = 9u << 20;
constexpr unsigned WS_BYTES = 17u << 20;
constexpr float C2 = 0.27264234f;
constexpr float LN_EPS = 1e-5f;
constexpr int AUX_SC1 = 16;
constexpr unsigned LDS_BYTES = 160 * 1024;
constexpr unsigned SPIN_MAX = 200000;
constexpr unsigned OOB = 0x7ffffff0u;
#define SAFE_BOUND 40.f
#define QD 1
#define QB 1
#define KQC 1
#define QX 1
#define QPF 3
#define PUBS 18
#define OUT_AUX 0
#define XL 1
#define XB 1
#define XA 1

DI rsrc_t mk_rsrc(const void* p, unsigned bytes) { return __builtin_amdgcn_make_buffer_rsrc((void*)p, (short)0, (int)bytes, 0x00020000); }
DI unsigned rfl(unsigned x) { return __builtin_amdgcn_readfirstlane(x); }
DI unsigned rfl_pad(unsigned x) { unsigned r = rfl(x); asm volatile("s_nop 4" : "+s"(r)); return r; }
DI void dma16(rsrc_t rs, unsigned voff, unsigned soff, unsigned ldsaddr) {
    asm volatile("s_mov_b32 m0, %0\n\ts_nop 0\n\tbuffer_load_dwordx4 %1, %2, %3 offen lds" ::"s"(ldsaddr), "v"(voff), "s"(rs), "s"(soff) : "memory");
}
DI void dma16_sc1(rsrc_t rs, unsigned voff, unsigned soff, unsigned ldsaddr) {
    asm volatile("s_mov_b32 m0, %0\n\ts_nop 0\n\tbuffer_load_dwordx4 %1, %2, %3 offen sc1 lds" ::"s"(ldsaddr), "v"(voff), "s"(rs), "s"(soff) : "memory");
}
#define WAIT_VM0() asm volatile("s_waitcnt vmcnt(0)" ::: "memory")
#define BAR() asm volatile("s_waitcnt lgkmcnt(0)\n\ts_barrier" ::: "memory")
#define VM0_BAR() asm volatile("s_waitcnt vmcnt(0) lgkmcnt(0)\n\ts_barrier" ::: "memory")
#define VMCNT(n) asm volatile("s_waitcnt vmcnt(%0)" ::"n"(n) : "memory")
#define VMCNT_BAR(n) asm volatile("s_waitcnt vmcnt(%0) lgkmcnt(0)\n\ts_barrier" ::"n"(n) : "memory")
#define SBAR() __builtin_amdgcn_sched_barrier(0)
#define PIN(x) asm volatile("" : "+v"(x))

template <int V> using IC = std::integral_constant<int, V>;
template <int... Is, class F> DI void static_for_(std::integer_sequence<int, Is...>, F&& f) { (f(IC<Is>{}), ...); }
template <int N, class F> DI void static_for(F&& f) { static_for_(std::make_integer_sequence<int, N>{}, f); }

DI f32x16 mfma16(u32x4 a, u32x4 b, f32x16 c) { return __builtin_amdgcn_mfma_f32_32x32x16_bf16(__builtin_bit_cast(bf16x8, a), __builtin_bit_cast(bf16x8, b), c, 0, 0, 0); }
DI f32x16 mfma_f32(float a, float b, f32x16 c) { return __builtin_amdgcn_mfma_f32_32x32x2f32(a, b, c, 0, 0, 0); }
DI unsigned cvtpk(float lo, float hi) { f32x2 v = {lo, hi}; return __builtin_bit_cast(unsigned, __builtin_convertvector(v, bf16x2)); }
DI float dot2(unsigned a, unsigned b, float c) { return __builtin_amdgcn_fdot2_f32_bf16(__builtin_bit_cast(bf16x2, a), __builtin_bit_cast(bf16x2, b), c, false); }
DI float swap_add(float x) { auto r = __builtin_amdgcn_permlane32_swap(__float_as_uint(x), __float_as_uint(x), false, false); return __uint_as_float(r[0]) + __uint_as_float(r[1]); }
DI float swap_max(float x) { auto r = __builtin_amdgcn_permlane32_swap(__float_as_uint(x), __float_as_uint(x), false, false); return fmaxf(__uint_as_float(r[0]), __uint_as_float(r[1])); }
template <int CTRL> DI float dpp_max(float x) { return fmaxf(x, __uint_as_float(__builtin_amdgcn_update_dpp(0u, __float_as_uint(x), CTRL, 0xf, 0xf, true))); }
DI float max32(float x) {
    x = dpp_max<0x128>(x); x = dpp_max<0x124>(x); x = dpp_max<0x122>(x); x = dpp_max<0x121>(x);
    auto sw = __builtin_amdgcn_permlane16_swap(__float_as_uint(x), __float_as_uint(x), false, false);
    return fmaxf(__uint_as_float(sw[0]), __uint_as_float(sw[1]));
}
template <int D2> DI u32x4 tr_frag(LAS char* p) {
    s16x4 lo = __builtin_amdgcn_ds_read_tr16_b64_v4i16((LAS s16x4*)p), hi = __builtin_amdgcn_ds_read_tr16_b64_v4i16((LAS s16x4*)(p + D2));
    u32x2 a = __builtin_bit_cast(u32x2, lo), b = __builtin_bit_cast(u32x2, hi);
    return u32x4{a[0], a[1], b[0], b[1]};
}
DI u32x4 pack8(const f32x16& v, int s) { return u32x4{cvtpk(v[8 * s], v[8 * s + 1]), cvtpk(v[8 * s + 2], v[8 * s + 3]), cvtpk(v[8 * s + 4], v[8 * s + 5]), cvtpk(v[8 * s + 6], v[8 * s + 7])}; }
DI void st16(rsrc_t rs, unsigned off, u32x4 v) { __builtin_amdgcn_raw_buffer_store_b128(v, rs, off, 0, AUX_SC1); }
DI u32x4 ld16(rsrc_t rs, unsigned voff, unsigned soff) { return __builtin_amdgcn_raw_buffer_load_b128(rs, voff, soff, AUX_SC1); }
DI float bf2f(unsigned short b) { return __uint_as_float((unsigned)b << 16); }
DI u64 ld_flag(const char* p) { return __hip_atomic_load((GAS const u64*)p, __ATOMIC_RELAXED, __HIP_MEMORY_SCOPE_AGENT); }
DI void st_flag(char* p, u64 v) { __hip_atomic_store((GAS u64*)p, v, __ATOMIC_RELAXED, __HIP_MEMORY_SCOPE_AGENT); }
DI void poll_flags(const char* p, bool use, u64 tag) {
    for (unsigned n = 0; n < SPIN_MAX; ++n) {
        const u64 v = use ? ld_flag(p) : tag;
        if (__all(v == tag)) break;
        __builtin_amdgcn_s_sleep(1);
    }
    asm volatile("" ::: "memory");
}
template <class T> DI T* opaque(T* p) { asm volatile("" : "+v"(p)); return p; }
#define LDF(p) (*(LAS u32x4*)(p))

#define STAMP(i) do {} while (0)
#define FSTAMP(i) do {} while (0)
struct Ptrs { const void* x; const void* wqkv; const void* wo; const void* w1; const void* b1; const void* w2; const void* b2; void* out; char* ws; };

constexpr unsigned L_STAT = 155648, L_CS = 156672;
DI void sum8(const u32x4& f, float& a, float& b) {
    float t0, t1, t2, t3;
    asm volatile("v_lshlrev_b32 %2, 16, %6\n\tv_and_b32 %3, 0xffff0000, %6\n\tv_lshlrev_b32 %4, 16, %7\n\tv_and_b32 %5, 0xffff0000, %7\n\t"
                 "v_add_f32 %0, %0, %2\n\tv_add_f32 %1, %1, %3\n\tv_add_f32 %0, %0, %4\n\tv_add_f32 %1, %1, %5\n\t"
                 "v_lshlrev_b32 %2, 16, %8\n\tv_and_b32 %3, 0xffff0000, %8\n\tv_lshlrev_b32 %4, 16, %9\n\tv_and_b32 %5, 0xffff0000, %9\n\t"
                 "v_add_f32 %0, %0, %2\n\tv_add_f32 %1, %1, %3\n\tv_add_f32 %0, %0, %4\n\tv_add_f32 %1, %1, %5"
                 : "+v"(a), "+v"(b), "=&v"(t0), "=&v"(t1), "=&v"(t2), "=&v"(t3) : "v"(f[0]), "v"(f[1]), "v"(f[2]), "v"(f[3]));
}
DI void gelu_a(float a0, float a1, float rstd, float c0, float& x0, float& x1, float& e0, float& e1) {
    asm volatile("v_mul_f32 %0, %4, %6\n\tv_mul_f32 %1, %5, %6\n\tv_mul_f32 %2, %0, %0\n\tv_mul_f32 %3, %1, %1\n\t"
                 "v_fmamk_f32 %2, %2, 0x3dd2d3e8, %7\n\tv_fmamk_f32 %3, %3, 0x3dd2d3e8, %7\n\tv_mul_f32 %2, %0, %2\n\tv_mul_f32 %3, %1, %3\n\t"
                 "v_exp_f32 %2, -%2\n\tv_exp_f32 %3, -%3"
                 : "=&v"(x0), "=&v"(x1), "=&v"(e0), "=&v"(e1) : "v"(a0), "v"(a1), "v"(rstd), "v"(c0));
}
DI unsigned gelu_b(float x0, float x1, float e0, float e1) {
    unsigned r;
    asm volatile("v_add_f32 %1, 1.0, %1\n\tv_add_f32 %2, 1.0, %2\n\tv_rcp_f32 %1, %1\n\tv_rcp_f32 %2, %2\n\ts_nop 0\n\tv_mul_f32 %1, %3, %1\n\tv_mul_f32 %2, %4, %2\n\tv_cvt_pk_bf16_f32 %0, %1, %2"
                 : "=v"(r), "+v"(e0), "+v"(e1) : "v"(x0), "v"(x1));
    return r;
}
#define MFMA_F32_PAD(x) asm volatile("s_nop 15\n\ts_nop 3" : "+v"(x))
DI float fmul_s(float a, float b) { float r; asm("v_mul_f32 %0, %1, %2" : "=v"(r) : "v"(a), "v"(b)); return r; }
DI float ffma_s(float a, float b, float c) { float r; asm("v_fma_f32 %0, %1, %2, %3" : "=v"(r) : "v"(a), "v"(b), "v"(c)); return r; }
DI float fadd_s(float a, float b) { float r; asm("v_add_f32 %0, %1, %2" : "=v"(r) : "v"(a), "v"(b)); return r; }
struct P1Dma { rsrc_t rqkv, rw1, rw2, rwo; unsigned vq0, vq1, vq2, vf, qsoff, qdst, fsoff, fdst, tsoff, tdst, usoff, udst, tdummy, vt2, vto; };

struct QkvAddr { LAS char* qa[4]; LAS char* qa2[4]; LAS char* xu[4]; };
template <int HALF, bool SWA, bool SWB>
DI void p1_qkv_pass(const Ptrs& P, const P1Dma& D, u32x4 (&xf)[28], const QkvAddr& A, bool is28, f32x16& accA, f32x16& accB, float& csA, float& csB) {
    constexpr int PF = 3;
    u32x4 wa[PF], wb[PF];
    float a0 = 0.f, a1 = 0.f, b0 = 0.f, b1 = 0.f;
    const unsigned ones = 0x3F803F80u;
    accA = f32x16{}; accB = f32x16{};
    auto rd = [&](auto SC) {
        constexpr int s = decltype(SC)::value; constexpr int o = s < 12 ? (s & ~3) * 3072 : ((s & ~3) - 12) * 3072;
        wa[s % PF] = tr_frag<768>((s < 12 ? A.qa[s & 3] : A.qa2[s & 3]) + o); if (HALF == 0) wb[s % PF] = tr_frag<768>((s < 12 ? A.qa[s & 3] : A.qa2[s & 3]) + o + 56);
    };
    FSTAMP(16);
    rd(IC<0>{}); rd(IC<1>{}); rd(IC<2>{});
    SBAR();
    static_for<28>([&](auto SC) {
        constexpr int s = decltype(SC)::value;
        if constexpr (s == 20) FSTAMP(25);
        u32x4 ca = wa[s % PF]; const u32x4 cb = wb[s % PF];
        if (HALF == 1) { ca[0] = is28 ? ones : ca[0]; ca[1] = is28 ? ones : ca[1]; ca[2] = is28 ? ones : ca[2]; ca[3] = is28 ? ones : ca[3]; }
        accA = SWA ? mfma16(xf[s], ca, accA) : mfma16(ca, xf[s], accA);
        if constexpr (!QD) {} else if constexpr (s < 6) dma16(D.rqkv, s % 3 == 0 ? D.vq0 : s % 3 == 1 ? D.vq1 : D.vq2, D.qsoff + (s / 3) * (16 * 2688), D.qdst + s * 1024);
        else if constexpr (s >= 8 && s < 16) dma16(D.rw1, D.vf, D.fsoff + (s - 8) * (4 * 3584), D.fdst + (s - 8) * 1024);
        else if constexpr (s >= 16 && s < 20) dma16(D.rw1, D.vf, D.tsoff + (s - 16) * (4 * 3584), D.tdst + (s - 16) * 1024);
        if (HALF == 0) accB = SWB ? mfma16(xf[s], cb, accB) : mfma16(cb, xf[s], accB);
        else accB = mfma16(xf[s], xf[s], accB);
        if constexpr (s + PF < 28 && !(s + PF >= 12 && s <= 11)) rd(IC<(s + PF < 28 ? s + PF : 0)>{});
        if constexpr (QX && s >= 4 && s < 8) {
#pragma unroll
            for (int i = 0; i < 3; ++i) { constexpr int dummy = 0; (void)dummy; const int j = 3 * (s - 4) + i; xf[16 + j] = LDF(A.xu[j & 3] + 128 * (j >> 2)); }
        }
        if constexpr (KQC && (s & 3) == 0) { sum8(ca, a0, a1); if (HALF == 0) sum8(cb, b0, b1); }
        SBAR();
        if constexpr (s == 12) {
            asm volatile("s_mov_b32 m0, %0\n\ts_nop 0\n\tbuffer_load_dword %1, %2, 0 offen lds\n\tbuffer_load_dword %3, %4, 0 offen lds" ::"s"(D.tdummy), "v"(D.vt2), "s"(D.rw2), "v"(D.vto), "s"(D.rwo) : "memory");
        }
        if constexpr (QB && s == 3) { VMCNT_BAR(6); STAMP(18); SBAR(); }
        if constexpr (QB && s == 7) { FSTAMP(19); BAR(); FSTAMP(20); STAMP(19); SBAR(); }
        if constexpr (s == 11) {
            FSTAMP(21); if (QB) VMCNT_BAR(4); FSTAMP(24); STAMP(20);
            rd(IC<12>{}); rd(IC<13>{}); rd(IC<14>{});
            SBAR();
        }
    });
    csA = a0 + a1; csB = b0 + b1;
}
DI void p1_ffn_pass(const Ptrs& P, LAS char* lds, const P1Dma& D, const u32x4 (&xf)[28], unsigned tt, unsigned off, f32x16& accA, f32x16& accB, float& csA, float& csB,
                    char* flag, u64 tag) {
    LAS char* q[4]; LAS char* q2[4]; LAS char* p[4]; LAS char* p2[4];
#pragma unroll
    for (int u = 0; u < 4; ++u) {
        const unsigned ro = off + ((u + tt) & 3) * 4096;
        q[u] = opaque(lds + 49152 + ro); q2[u] = opaque(lds + 114688 + ro); p[u] = opaque(lds + 49152 + (ro ^ 64)); p2[u] = opaque(lds + 114688 + (ro ^ 64));
    }
    constexpr int PF = 3;
    u32x4 wa[PF], wb[PF];
    float a0 = 0.f, a1 = 0.f, b0 = 0.f, b1 = 0.f;
    accA = f32x16{}; accB = f32x16{};
    auto rd = [&](auto SC) {
        constexpr int s = decltype(SC)::value;
        if constexpr (s < 16) { wa[s % PF] = tr_frag<1024>(q[s & 3] + (s & ~3) * 4096); wb[s % PF] = tr_frag<1024>(p[s & 3] + (s & ~3) * 4096); }
        else if constexpr (s < 24) { wa[s % PF] = tr_frag<1024>(q2[s & 3] + ((s & ~3) - 16) * 4096); wb[s % PF] = tr_frag<1024>(p2[s & 3] + ((s & ~3) - 16) * 4096); }
        else { wa[s % PF] = tr_frag<1024>(q[s & 3] - 49152); wb[s % PF] = tr_frag<1024>(p[s & 3] - 49152); }
    };
    rd(IC<0>{}); rd(IC<1>{}); rd(IC<2>{});
    SBAR();
    static_for<28>([&](auto SC) {
        constexpr int s = decltype(SC)::value;
        const u32x4 ca = wa[s % PF], cb = wb[s % PF];
        accA = mfma16(ca, xf[s], accA);
        accB = mfma16(cb, xf[s], accB);
        if constexpr (s + PF < 28) rd(IC<(s + PF < 28 ? s + PF : 0)>{});
        if constexpr ((s & 3) == 0) sum8(ca, a0, a1);
        if constexpr ((s & 3) == 0) sum8(cb, b0, b1);
        SBAR();
        if constexpr (s == 12) {
            VMCNT_BAR(8);
            STAMP(21);
            SBAR();
        }
        if constexpr (s == PUBS) {
            VMCNT_BAR(0);
            if (threadIdx.x == 0) st_flag(flag, tag);
            SBAR();
        }
    });
    csA = a0 + a1; csB = b0 + b1;
}
DI float p1_cs(LAS float* csb, unsigned l31) {
    float v[8];
#pragma unroll
    for (int i = 0; i < 8; ++i) v[i] = csb[i * 32 + l31];
    return ((v[0] + v[1]) + (v[2] + v[3])) + ((v[4] + v[5]) + (v[6] + v[7]));
}
struct P1Ctx { rsrc_t rws; char* ws; unsigned lane, l31, h, tile; float mu, sig, rstd; };
DI void p1_gelu(const P1Ctx& C, f32x16 acc, float cs, float bias, u32x4& pk0, u32x4& pk1) {
    acc = mfma_f32(C.h ? bias : cs, C.h ? C.sig : -C.mu, acc);
    const float k0 = 2.302208198f * C.rstd, k1 = 0.1029432397f * C.rstd * C.rstd * C.rstd;
#pragma unroll
    for (int i = 0; i < 16; i += 2) {
        const f32x2 a = f32x2{acc[i], acc[i + 1]}, z = a * __builtin_elementwise_fma(a * a, f32x2{k1, k1}, f32x2{k0, k0});
        const f32x2 d = __builtin_elementwise_fma(f32x2{__builtin_amdgcn_exp2f(-z[0]), __builtin_amdgcn_exp2f(-z[1])}, f32x2{C.sig, C.sig}, f32x2{C.sig, C.sig});
        const f32x2 y = a * f32x2{__builtin_amdgcn_rcpf(d[0]), __builtin_amdgcn_rcpf(d[1])};
        acc[i] = y[0]; acc[i + 1] = y[1];
    }
    pk0 = pack8(acc, 0); pk1 = pack8(acc, 1);
}
DI void p1_store_h(const P1Ctx& C, unsigned ft, u32x4 pk0, u32x4 pk1) {
    const unsigned off = WS_H + (C.tile * 112 + 2 * ft) * 1024 + C.lane * 16;
    st16(C.rws, off, pk0); st16(C.rws, off + 1024, pk1);
}
DI void p1_epi_qkv(const P1Ctx& C, f32x16 acc, float cs, unsigned type, unsigned head) {
    const unsigned h = C.h, l31 = C.l31;
    const bool isv = type == 2;
    const float m0 = h ? 0.f : -C.mu, c0 = h ? 0.f : cs, r0 = h ? 0.f : (type == 0 ? C.rstd * C2 : C.rstd), k0 = (h || l31 >= 28) ? 0.f : 1.f;
    acc = mfma_f32(isv ? m0 : c0, isv ? c0 : m0, acc);
    f32x16 R = {}; R = mfma_f32(isv ? r0 : k0, isv ? k0 : r0, R);
    const float pv = (isv && l31 == 28) ? 1.f : 0.f, p12 = isv ? pv : ((type == 1 && h) ? 1.f : 0.f);
    f32x2 n2 = {0.f, 0.f};
#pragma unroll
    for (int i = 0; i < 16; i += 2) {
        const f32x2 t = __builtin_elementwise_fma(f32x2{acc[i], acc[i + 1]}, f32x2{R[i], R[i + 1]}, f32x2{i == 12 ? p12 : pv, pv});
        acc[i] = t[0]; acc[i + 1] = t[1];
        n2 = __builtin_elementwise_fma(t, t, n2);
    }
    const unsigned off = WS_Q + type * (2u << 20) + (head * 64 + C.tile) * 2048 + C.lane * 16;
    st16(C.rws, off, pack8(acc, 0)); st16(C.rws, off + 1024, pack8(acc, 1));
    const float kn = max32(swap_add(n2[0] + n2[1]));
    __builtin_amdgcn_raw_buffer_store_b32(__float_as_uint(kn), C.rws, (type == 1 && C.lane == 0) ? WS_KN2 + (head * 64 + C.tile) * 4 : OOB, 0, AUX_SC1);
}

DI u64 make_tag(u64 e) {
    e = ((u64)rfl((unsigned)(e >> 32)) << 32) | rfl((unsigned)e);
    u64 tag = (e + 0x9E3779B97F4A7C15ull); tag = (tag ^ (tag >> 30)) * 0xBF58476D1CE4E5B9ull; tag = (tag ^ (tag >> 27)) * 0x94D049BB133111EBull; tag ^= tag >> 31;
    return tag;
}
constexpr unsigned p3_region(int sb) { return (sb % 3) * 49152u; }
constexpr unsigned RING = 98304;
constexpr unsigned ring_off(int c) { return c % 4 == 2 ? 0u : c % 4 == 0 ? 16384u : c % 4 == 1 ? 32768u : 49152u; }
struct P3W { rsrc_t rw2, rwo; unsigned vw, vo0, vo1, soff, dst, hd0, vt2, vto; };
template <int SB, int I> DI void p3_dma(const P3W& W) {
    constexpr int j = 6 * SB + I;
    if constexpr (j < 28) dma16(W.rw2, W.vw, W.soff + j * (16 * DM * 2), W.dst + p3_region(SB) + I * 2048);
    else {
        dma16(W.rwo, ((j - 28) & 1) ? W.vo1 : W.vo0, W.hd0 + ((j - 28) >> 1) * (28 * DM * 2), W.dst + p3_region(SB) + I * 2048);
    }
}
DI P3W make_p3w(const Ptrs& P, unsigned lds0, int b, unsigned w, unsigned lane) {
    const unsigned r = b >> 3, cg3 = r < 28 ? r >> 2 : 0;
    P3W W3;
    const unsigned vcol = cg3 * 128 + (((lane >> 2) & 1) ^ ((lane >> 4) & 1)) * 64 + (lane & 3) * 16, rw = (w & 1) * 8 + (lane >> 3);
    W3.rw2 = mk_rsrc(P.w2, FF * DM * 2); W3.rwo = mk_rsrc(P.wo, DM * DM * 2);
    W3.vw = rw * (DM * 2) + vcol; W3.vo0 = W3.vw; W3.vo1 = W3.vw + 16 * (DM * 2);
    W3.soff = rfl_pad(28 * (w >> 1) * (16 * DM * 2)); W3.hd0 = rfl_pad(4 * (w >> 1) * (28 * DM * 2));
    W3.dst = rfl_pad(lds0 + (w >> 1) * 6 * 2048 + (w & 1) * 1024);
    {
        const unsigned L = w * 64 + lane, j = 12 + (L >> 4), row = L & 15, qq = r & 3;
        W3.vt2 = w < 4 ? ((28 * qq + j) * 16 + row) * (DM * 2) + cg3 * 128 : OOB;
        W3.vto = (w == 4 || w == 5) ? (28 * (4 * qq + ((j - 28) >> 1)) + 16 * ((j - 28) & 1) + row) * (DM * 2) + cg3 * 128 : OOB;
    }
    return W3;
}
DI void kv_first(const Ptrs& P, LAS char* lds, int b, unsigned w, unsigned lane, u32x4& qf0, u32x4& qf1, float& kn2) {
    const unsigned g = b & 7, r = b >> 3, head = 2 * g + (r >> 4), tile = (r & 15) * 4 + (w & 3);
    const rsrc_t rws = mk_rsrc(P.ws, WS_BYTES);
    const unsigned src = rfl_pad(((w & 1) ? WS_V : WS_K) + (head * 64 + (w >> 2) * 32 + ((w >> 1) & 1)) * 2048), wdst = rfl_pad((unsigned)(size_t)lds + 98304 + w * 2048);
    dma16_sc1(rws, lane * 16, src, wdst + 16384); dma16_sc1(rws, lane * 16 + 1024, src, wdst + 16384 + 1024);
    dma16_sc1(rws, lane * 16, src + 4096, wdst + 32768); dma16_sc1(rws, lane * 16 + 1024, src + 4096, wdst + 32768 + 1024);
    const unsigned qs = rfl_pad(WS_Q + (head * 64 + tile) * 2048), ks = rfl_pad(WS_KN2 + head * 256);
    asm volatile("buffer_load_dwordx4 %0, %3, %5, %6 offen sc1\n\tbuffer_load_dwordx4 %1, %3, %5, %6 offen offset:1024 sc1\n\tbuffer_load_dword %2, %4, %5, %7 offen sc1"
                 : "=&v"(qf0), "=&v"(qf1), "=&v"(kn2) : "v"(lane * 16), "v"(lane * 4), "s"(rws), "s"(qs), "s"(ks) : "memory");
}
DI u64 phase1(const Ptrs& P, LAS char* lds, int b, u32x4& qf0, u32x4& qf1, float& kn2, P3W& W3) {
    const unsigned tid = threadIdx.x, lane = tid & 63, l31 = lane & 31, h = lane >> 5;
    const unsigned w = rfl(tid >> 6), tt = w & 3, half = w >> 2;
    const unsigned g = b & 7, r = b >> 3, tb = 4 * (g >> 1) + (r & 3), cg = 8 * (g & 1) + (r >> 2);
    const unsigned lds0 = (unsigned)(size_t)lds;
    const rsrc_t rx = mk_rsrc(P.x, SEQ * DM * 2);
    P1Dma D;
    D.rqkv = mk_rsrc(P.wqkv, DM * 3 * DM * 2);
    const unsigned cb = 168 * cg, cb16 = cb & ~15u, dl = cb & 15u;
    FSTAMP(13);
    {
        const unsigned rowa = 2 * w + h;
        const unsigned va = rowa * 896 + ((l31 ^ (rowa & 15)) << 4), sa = tb * 128 * 896;
#pragma unroll
        for (unsigned i = 0; i < 8; ++i) dma16(rx, va, sa + i * (16 * 896), lds0 + (w + 8 * i) * 1024);
    }
    {
        unsigned long long pc; unsigned len;
        asm volatile("s_getpc_b64 %0\n\tfk_code_here:\n\ts_mov_b32 %1, fk_code_end-fk_code_here" : "=s"(pc), "=s"(len));
        const unsigned per = (len / 128 + 31) / 32, line = r * per + w * 64 + lane;
        const bool on = w * 64 + lane < per && line * 128 < len;
        const unsigned long long a = pc + (on ? line * 128 : 0);
        asm volatile("s_mov_b32 m0, %0\n\ts_nop 0\n\tglobal_load_lds_dword %1, off" ::"s"(lds0 + 151552 + w * 256), "v"(a) : "memory");
    }
    {
        auto vq = [&](unsigned p) { const unsigned row = p / 12, ch = p - row * 12; return ch < 11 ? row * 2688 + cb16 + ch * 16 : OOB; };
        D.vq0 = vq(lane); D.vq1 = vq(64 + lane); D.vq2 = vq(128 + lane);
    }
    const unsigned ntile = ((cg ^ (cg >> 1)) & 1) ? 3 : 4, fstart = 7 * (cg >> 1) + ((cg & 1) ? (((cg >> 1) & 1) ? 3 : 4) : 0);
    D.rw1 = mk_rsrc(P.w1, DM * FF * 2);
    { const unsigned rq = lane >> 4, sl = ((lane >> 2) & 3) ^ rq; D.vf = sl < ntile ? rq * 3584 + (fstart + sl) * 64 + (lane & 3) * 16 : OOB; }
    D.qsoff = (12 + 2 * w) * (16 * 2688); D.qdst = lds0 + 2 * w * 3072;
    D.fsoff = 2 * w * (16 * 3584); D.fdst = lds0 + 49152 + 2 * w * 4096;
    D.tsoff = (256 + 16 * w) * 3584; D.tdst = lds0 + 114688 + w * 4096;
    D.usoff = (384 + 8 * w) * 3584; D.udst = lds0 + 2 * w * 1024;
    D.rw2 = mk_rsrc(P.w2, FF * DM * 2); D.rwo = mk_rsrc(P.wo, DM * DM * 2); D.tdummy = lds0 + 151552 + w * 256;
    { const unsigned i = w * 8 + (lane & 7); D.vt2 = (lane < 8 && i < 49) ? (b * 49 + i) * 128 : OOB; D.vto = (lane < 8 && i < 13) ? (b * 13 + i) * 128 : OOB; }
    const u64 eraw = ld_flag(P.ws + WS_EPOCH);
    const unsigned short b1a = ((const unsigned short*)P.b1)[(fstart + 2 * half) * 32 + l31];
    const unsigned short b1b = ((const unsigned short*)P.b1)[(2 * half + 1 < ntile ? fstart + 2 * half + 1 : fstart) * 32 + l31];
    {
        const unsigned so = w * (16 * 2688), ds = lds0 + 114688 + w * 3072;
        if (w < 4) { dma16(D.rqkv, D.vq0, so, ds); dma16(D.rqkv, D.vq1, so, ds + 1024); dma16(D.rqkv, D.vq2, so, ds + 2048); }
        dma16(D.rqkv, D.vq0, so + 4 * (16 * 2688), ds + 4 * 3072); dma16(D.rqkv, D.vq1, so + 4 * (16 * 2688), ds + 4 * 3072 + 1024); dma16(D.rqkv, D.vq2, so + 4 * (16 * 2688), ds + 4 * 3072 + 2048);
    }
#pragma unroll
    for (unsigned i = 0; i < 6; ++i) {
        const unsigned p = 64 * i + lane, rr = p / 24, c = p - rr * 24, row = 16 * w + rr;
        dma16(rx, row * 896 + 512 + ((c ^ ((row >> 1) & 7)) << 4), tb * 128 * 896, lds0 + 65536 + (6 * w + i) * 1024);
    }
    const rsrc_t rws = mk_rsrc(P.ws, WS_BYTES);
    LAS float* stat = (LAS float*)(lds + L_STAT);
    LAS float* csb = (LAS float*)(lds + L_CS);
    P1Ctx C{rws, P.ws, lane, l31, h, tb * 4 + tt, 0.f, 0.f, 0.f};
    const unsigned G = lane >> 4, q4 = (lane & 15) >> 2;
    const unsigned trq = (8 * (G >> 1) + q4) * 192 + dl + (G & 1) * 32 + (lane & 3) * 8 + half * 112;
    const unsigned trf = (8 * (G >> 1) + q4) * 256 + (G & 1) * 32 + (lane & 3) * 8;
    f32x16 accA, accB; float csA, csB;
    const unsigned tA = 3 * cg + 2 * half, tB = 3 * cg + 1, tyA = tA >> 4, tyB = tB >> 4;
    QkvAddr QA;
    {
        LAS char* xb0 = lds + 65536 + (tt * 32 + l31) * 384; const unsigned gswb = (l31 >> 1) & 7;
#pragma unroll
        for (int u = 0; u < 4; ++u) {
            const unsigned ro = ((u + tt) & 3) * 3072;
            QA.qa[u] = opaque(lds + 114688 + trq + ro); QA.qa2[u] = opaque(lds + trq + ro);
            QA.xu[u] = opaque(xb0 + (((2 * ((u + tt) & 3) + h) ^ gswb) << 4));
        }
    }
    const bool m28 = l31 == 28;
    {
        const unsigned i5 = w * 5 + lane, i7 = w * 7 + lane, g5 = b * 37 + i5;
        const unsigned v5 = (lane < 5 && i5 < 37 && g5 < 9408) ? g5 * 128 : OOB, v7 = (lane < 7 && i7 < 49) ? (b * 49 + i7) * 128 : OOB;
        asm volatile("s_mov_b32 m0, %0\n\ts_nop 0\n\tbuffer_load_dword %1, %2, 0 offen lds\n\tbuffer_load_dword %3, %4, 0 offen lds" ::"s"(D.tdummy), "v"(v5), "s"(D.rqkv), "v"(v7), "s"(D.rw1) : "memory");
    }
    FSTAMP(14);
    VMCNT_BAR(11);
    STAMP(1);
    u32x4 xf[28];
    {
        LAS char* xa = lds + (tt * 32 + l31) * 512;
#pragma unroll
        for (int u = 0; u < 4; ++u) {
            const unsigned c = 2 * ((u + tt) & 3) + h;
            LAS char* x0 = xa + ((c ^ (l31 & 15)) << 4); LAS char* x1 = xa + (((c + 8) ^ (l31 & 15)) << 4);
            xf[u] = LDF(x0); xf[4 + u] = LDF(x1); xf[8 + u] = LDF(x0 + 256); xf[12 + u] = LDF(x1 + 256);
        }
    }
    const u64 tag = make_tag(eraw);
    VMCNT_BAR(8);
    STAMP(2);
    if (half == 0) {
        if (tyA == 2) p1_qkv_pass<0, true, true>(P, D, xf, QA, m28, accA, accB, csA, csB);
        else if (tyB == 2) p1_qkv_pass<0, false, true>(P, D, xf, QA, m28, accA, accB, csA, csB);
        else p1_qkv_pass<0, false, false>(P, D, xf, QA, m28, accA, accB, csA, csB);
        csb[0 * 256 + tt * 64 + lane] = csA; csb[1 * 256 + tt * 64 + lane] = csB;
    } else {
        float a1;
        if (tyA == 2) {
            p1_qkv_pass<1, true, false>(P, D, xf, QA, m28, accA, accB, csA, csB);
            if (l31 == 28) {
#pragma unroll
                for (int i = 0; i < 16; ++i) stat[(tt * 32 + (i & 3) + 8 * (i >> 2) + 4 * h) * 2] = accA[i];
            }
            asm volatile("s_waitcnt lgkmcnt(0)" ::: "memory");
            a1 = stat[(tt * 32 + l31) * 2];
        } else {
            p1_qkv_pass<1, false, false>(P, D, xf, QA, m28, accA, accB, csA, csB);
            auto sw = __builtin_amdgcn_permlane32_swap(__float_as_uint(accA[12]), __float_as_uint(accA[12]), false, false);
            a1 = __uint_as_float(sw[1]);
        }
        csb[2 * 256 + tt * 64 + lane] = csA;
        const unsigned ri = (l31 & 3) + 4 * (l31 >> 3);
        float dg = 0.f;
#pragma unroll
        for (int i = 0; i < 16; ++i) dg = ri == (unsigned)i ? accB[i] : dg;
        const float a2 = swap_add(h == ((l31 >> 2) & 1) ? dg : 0.f);
        const float m = a1 * (1.f / DM), var = fmaxf(a2 * (1.f / DM) - m * m, 0.f) + LN_EPS;
        if (!h) *(LAS f32x2*)(stat + (tt * 32 + l31) * 2) = f32x2{m, __builtin_amdgcn_rsqf(var)};
    }
    FSTAMP(2);
    VMCNT_BAR(4);
    STAMP(3);
    dma16(D.rw1, D.vf, D.usoff, D.udst); dma16(D.rw1, D.vf, D.usoff + 4 * 3584, D.udst + 1024);
    { const f32x2 st = *(LAS f32x2*)(stat + (tt * 32 + l31) * 2); C.mu = st[0]; C.rstd = st[1]; C.sig = __builtin_amdgcn_rcpf(st[1]); }
    {
        const float c0 = p1_cs(csb + 2 * half * 256, l31), c1 = p1_cs(csb + 256, l31);
        p1_epi_qkv(C, accA, c0, tyA, tA & 15);
        if (half == 0) p1_epi_qkv(C, accB, c1, tyB, tB & 15);
        else asm volatile("buffer_store_dword %0, %0, %1, 0 offen sc1\n\tbuffer_store_dword %0, %0, %1, 0 offen sc1\n\tbuffer_store_dword %0, %0, %1, 0 offen sc1" ::"v"(OOB), "s"(rws) : "memory");
    }
    FSTAMP(5);
    STAMP(4);
    const unsigned head2 = 2 * g + (r >> 4), qb2 = r & 15;
    const char* qflag;
    { const unsigned cq = head2 / 3, ck = (16 + head2) / 3, cv = (32 + head2) / 3;
      const unsigned idx = lane < 16 ? ck * 16 + lane : lane < 32 ? cv * 16 + (lane - 16) : cq * 16 + qb2; qflag = P.ws + WS_FQKV + (lane <= 32 ? idx : 0) * 64; }
    p1_ffn_pass(P, lds, D, xf, tt, trf + 64 * ((2 * half) ^ q4), accA, accB, csA, csB, P.ws + WS_FQKV + (cg * 16 + tb) * 64, tag);
    csb[(3 + 2 * half) * 256 + tt * 64 + lane] = csA; csb[(4 + 2 * half) * 256 + tt * 64 + lane] = csB;
    W3 = make_p3w(P, lds0, b, w, lane);
    STAMP(22);
    BAR();
    STAMP(23);
    {
        const bool second = 2 * half + 1 < ntile;
        const float c0 = p1_cs(csb + (3 + 2 * half) * 256, l31), c1 = p1_cs(csb + (4 + 2 * half) * 256, l31);
        u32x4 pk0, pk1;
        FSTAMP(27);
        poll_flags(qflag, lane <= 32, tag);
        FSTAMP(28);
        kv_first(P, lds, b, w, lane, qf0, qf1, kn2);
        FSTAMP(29);
        p1_gelu(C, accA, c0, bf2f(b1a), pk0, pk1); p1_store_h(C, fstart + 2 * half, pk0, pk1);
        if (second) { p1_gelu(C, accB, c1, bf2f(b1b), pk0, pk1); p1_store_h(C, fstart + 2 * half + 1, pk0, pk1); }
        else { asm volatile("buffer_store_dword %0, %0, %1, 0 offen sc1\n\tbuffer_store_dword %0, %0, %1, 0 offen sc1" ::"v"(OOB), "s"(rws) : "memory"); }
    }
    FSTAMP(12);
    STAMP(6);
    return tag;
}

constexpr int att_aflo(int T, bool PF) { return (!PF || T < 6) ? 0 : (T - 6) * 28 / 26; }
constexpr int att_kv(int T) { return (XL && (T & 1) == 0 && (T >> 1) + 3 < 16) ? 2 : 0; }
constexpr int att_w(int T, bool PF) { return !PF ? 0 : T < 12 ? 1 : T == 28 ? 2 : 0; }
constexpr int att_all(int T0, int T1, bool PF) { int n = 0; for (int T = T0; T < T1; ++T) n += att_kv(T) + att_w(T, PF) + (att_aflo(T + 1, PF) - att_aflo(T, PF)); return n; }
#define EXP4(S, i) do { S[i] = __builtin_amdgcn_exp2f(S[i]); S[i + 1] = __builtin_amdgcn_exp2f(S[i + 1]); S[i + 2] = __builtin_amdgcn_exp2f(S[i + 2]); S[i + 3] = __builtin_amdgcn_exp2f(S[i + 3]); } while (0)
template <int KH, bool PF>
DI void att_fast(LAS char* lds, rsrc_t rws, const P3W& W3, unsigned lane, unsigned w, unsigned src, unsigned wdst, unsigned afsrc, u32x4 qf0, u32x4 qf1, f32x16& o, u32x4 (&af)[28], const char* hflag, char* myh, u64 tag, unsigned& tch0, unsigned& tch1) {
    LAS char* kvb = opaque(lds + RING + KH * 8192 + lane * 16);
    const unsigned vo = lane * 16, vo1 = lane * 16 + 1024;
    src = rfl_pad(src); wdst = rfl_pad(wdst); afsrc = rfl_pad(afsrc);
    const f32x16 zero = {};
    f32x16 sc[2];
    u32x4 k0 = LDF(kvb + ring_off(0)), k1 = LDF(kvb + ring_off(0) + 1024), v0 = LDF(kvb + ring_off(0) + 2048), v1 = LDF(kvb + ring_off(0) + 3072), p1 = {0u, 0u, 0u, 0u}, v1p = {0u, 0u, 0u, 0u};
    sc[0] = mfma16(k0, qf0, zero); sc[0] = mfma16(k1, qf1, sc[0]);
    k0 = LDF(kvb + ring_off(0) + 4096); k1 = LDF(kvb + ring_off(0) + 4096 + 1024);
    static_for<32>([&](auto TC) {
        constexpr int T = decltype(TC)::value, c = T >> 1;
        f32x16& S = sc[T & 1]; f32x16& SN = sc[(T + 1) & 1];
        constexpr int o1 = ring_off((T + 1) >> 1) + ((T + 1) & 1) * 4096, o2 = ring_off((T + 2) >> 1) + ((T + 2) & 1) * 4096;
        u32x4 p0, k0n, k1n, v0n, v1n;
        auto chunk_barrier = [&](auto OPS) {
            constexpr int younger = decltype(OPS)::value;
            if constexpr (PF && T == 5 && KH == 0) { if (w == 0) { PIN(p1); PIN(o); poll_flags(hflag, lane < 16, tag); } }
            if (XB) asm volatile("s_waitcnt vmcnt(%0) lgkmcnt(%1)\n\ts_barrier" ::"n"(younger), "n"(KH == 0 ? 4 : 2) : "memory");
            if constexpr (T == 1 && KH == 0) { if (w == 0) { PIN(p1); PIN(o); if (lane == 0) st_flag(myh, tag); } }
        };
        S[0] = __builtin_amdgcn_exp2f(S[0]); S[1] = __builtin_amdgcn_exp2f(S[1]);
        if (T > 0) o = mfma16(v1p, p1, o);
        S[2] = __builtin_amdgcn_exp2f(S[2]); S[3] = __builtin_amdgcn_exp2f(S[3]);
        if (T + 2 < 32) { k0n = LDF(kvb + o2); k1n = LDF(kvb + o2 + 1024); }
        p0[0] = cvtpk(S[0], S[1]); p0[1] = cvtpk(S[2], S[3]); SBAR();
        if (T + 1 < 32) SN = mfma16(k0, qf0, zero);
        EXP4(S, 4);
        if constexpr (att_kv(T) != 0) { dma16_sc1(rws, vo, src + (c + 3) * 4096, wdst + ring_off(c + 3)); dma16_sc1(rws, vo1, src + (c + 3) * 4096, wdst + ring_off(c + 3) + 1024); }
        if constexpr (PF && T < 12) p3_dma<T / 6, T % 6>(W3);
        if constexpr (PF && T == 28) { tch0 = __builtin_amdgcn_raw_buffer_load_b32(W3.rw2, W3.vt2, 0, 0); tch1 = __builtin_amdgcn_raw_buffer_load_b32(W3.rwo, W3.vto, 0, 0); }
        static_for<att_aflo(T + 1, PF) - att_aflo(T, PF)>([&](auto JC) { constexpr int j = att_aflo(T, PF) + decltype(JC)::value; af[j] = ld16(rws, vo + (j & 3) * 1024, afsrc + (j >> 2) * 4096); });
        p0[2] = cvtpk(S[4], S[5]); p0[3] = cvtpk(S[6], S[7]); SBAR();
        constexpr int younger = c >= 1 ? att_all(2 * c - 2, T + 1, PF) - att_kv(2 * c - 2) : att_all(0, T + 1, PF);
        if constexpr (KH == 1 && (T & 1) == 1 && T < 31) chunk_barrier(IC<younger>{});
        if (T + 1 < 32) SN = mfma16(k1, qf1, SN);
        EXP4(S, 8);
        if (T + 1 < 32) { v0n = LDF(kvb + o1 + 2048); v1n = LDF(kvb + o1 + 3072); }
        p1[0] = cvtpk(S[8], S[9]); p1[1] = cvtpk(S[10], S[11]); SBAR();
        o = mfma16(v0, p0, o);
        EXP4(S, 12);
        p1[2] = cvtpk(S[12], S[13]); p1[3] = cvtpk(S[14], S[15]); SBAR();
        v1p = v1;
        if (T + 2 < 32) { k0 = k0n; k1 = k1n; }
        if (T + 1 < 32) { v0 = v0n; v1 = v1n; }
        if constexpr (KH == 0 && (T & 1) == 1 && T < 31) chunk_barrier(IC<younger>{});
    });
    o = mfma16(v1p, p1, o);
}

DI void phase23(const Ptrs& P, LAS char* lds, int b, u64 tag, u32x4 qf0, u32x4 qf1, float kn2, const P3W& W3) {
    const unsigned tid = threadIdx.x, lane = tid & 63, l31 = lane & 31, h = lane >> 5;
    const unsigned w = rfl(tid >> 6), qt = w & 3, kh = w >> 2;
    const unsigned g = b & 7, r = b >> 3, head = 2 * g + (r >> 4), qb = r & 15, tile = qb * 4 + qt;
    const unsigned lds0 = (unsigned)(size_t)lds;
    const rsrc_t rws = mk_rsrc(P.ws, WS_BYTES);
    const bool p3 = r < 28;
    const unsigned tt3 = w & 1, q3 = w >> 1, tb3 = 4 * g + (r & 3), cg3 = p3 ? r >> 2 : 0, tile3 = tb3 * 2 + tt3;
    STAMP(7);
    const unsigned src = rfl_pad(((w & 1) ? WS_V : WS_K) + (head * 64 + kh * 32 + ((w >> 1) & 1)) * 2048);
    const unsigned wdst = rfl_pad(lds0 + RING + w * 2048);
#define STAGE(c, off) do { dma16_sc1(rws, lane * 16, src + (c) * 4096, wdst + (off)); dma16_sc1(rws, lane * 16 + 1024, src + (c) * 4096, wdst + (off) + 1024); } while (0)
    asm volatile("s_waitcnt vmcnt(4)" : "+v"(qf0), "+v"(qf1), "+v"(kn2)::"memory");
    kn2 = swap_max(max32(kn2));
    float qn2 = 0.f;
#pragma unroll
    for (int e = 0; e < 4; ++e) { qn2 = dot2(qf0[e], qf0[e], qn2); qn2 = dot2(qf1[e], qf1[e], qn2); }
    qn2 = swap_add(qn2);
    const float bound = __builtin_amdgcn_sqrtf(qn2 * kn2) * 1.01f + 0.01f;
    const bool safe = __all(bound <= SAFE_BOUND);
    float mrun;
    if (safe) {
        const unsigned nb = cvtpk(-bound, 0.f) & 0xffffu;
        if (h) qf1[2] = (qf1[2] & 0xffff0000u) | nb;
        mrun = __uint_as_float((nb & 0x7fffu) << 16);
    } else mrun = -INFINITY;
    PIN(qf1); PIN(mrun);
    STAGE(2, ring_off(2));
    BAR();
    char* myh = P.ws + WS_FH + ((4 * (g >> 1) + (r & 3)) * 16 + 8 * (g & 1) + (r >> 2)) * 64;
    STAMP(8);
    f32x16 o = {};
    u32x4 af[28];
    unsigned tch0 = 0, tch1 = 0;
    const unsigned afsrc = WS_H + (tile3 * 112 + 28 * q3) * 1024;
    const char* hflag = P.ws + WS_FH + ((tb3 >> 1) * 16 + (lane & 15)) * 64;
    if (safe) {
        if (p3) {
            if (kh == 0) att_fast<0, true>(lds, rws, W3, lane, w, src, wdst, afsrc, qf0, qf1, o, af, hflag, myh, tag, tch0, tch1);
            else att_fast<1, true>(lds, rws, W3, lane, w, src, wdst, afsrc, qf0, qf1, o, af, hflag, myh, tag, tch0, tch1);
        } else {
            if (kh == 0) att_fast<0, false>(lds, rws, W3, lane, w, src, wdst, afsrc, qf0, qf1, o, af, hflag, myh, tag, tch0, tch1);
            else att_fast<1, false>(lds, rws, W3, lane, w, src, wdst, afsrc, qf0, qf1, o, af, hflag, myh, tag, tch0, tch1);
        }
    } else {
        const f32x16 zero = {};
        LAS char* kvb = lds + RING + kh * 8192 + lane * 16;
        static_for<12>([&](auto TC) { constexpr int T = decltype(TC)::value; p3_dma<T / 6, T % 6>(W3); });
#pragma unroll 1
        for (unsigned c = 0; c < 16; ++c) {
            auto roff = [](unsigned c) { return (c & 3) == 2 ? 0u : (c & 3) == 0 ? 16384u : (c & 3) == 1 ? 32768u : 49152u; };
            if (c + 3 < 16) STAGE(c + 3, roff(c + 3));
#pragma unroll 1
            for (int t = 0; t < 2; ++t) {
                LAS char* kv = kvb + roff(c) + t * 4096;
                const u32x4 k0 = LDF(kv), k1 = LDF(kv + 1024), v0 = LDF(kv + 2048), v1 = LDF(kv + 3072);
                f32x16 s = mfma16(k0, qf0, zero); s = mfma16(k1, qf1, s);
                float m = s[0];
#pragma unroll
                for (int i = 1; i < 16; ++i) m = fmaxf(m, s[i]);
                m = fmaxf(swap_max(m), mrun);
                const float f = __builtin_amdgcn_exp2f(mrun - m);
                mrun = m;
#pragma unroll
                for (int i = 0; i < 16; ++i) { s[i] = __builtin_amdgcn_exp2f(s[i] - m); o[i] *= f; }
                o = mfma16(v0, pack8(s, 0), o); o = mfma16(v1, pack8(s, 1), o);
            }
            if (c == 2 && w == 0) poll_flags(hflag, lane < 16, tag);
            if (c < 15) VM0_BAR();
            if (c == 0 && tid == 0) st_flag(myh, tag);
        }
#pragma unroll
        for (int j = 0; j < 28; ++j) af[j] = ld16(rws, lane * 16, afsrc + j * 1024);
    }
    BAR();
    STAMP(9);
    LAS float* ex = (LAS float*)(lds + 147456 + qt * 4096);
    if (kh == 1) {
        if (h) o[15] = mrun;
#pragma unroll
        for (int i = 0; i < 16; ++i) ex[i * 64 + lane] = o[i];
    }
    BAR();
    if (kh == 0) {
        const float m2 = ex[15 * 64 + 32 + l31], m = fmaxf(mrun, m2), f1 = __builtin_amdgcn_exp2f(mrun - m), f2 = __builtin_amdgcn_exp2f(m2 - m);
#pragma unroll
        for (int i = 0; i < 16; ++i) o[i] = o[i] * f1 + ex[i * 64 + lane] * f2;
        auto sw = __builtin_amdgcn_permlane32_swap(__float_as_uint(o[12]), __float_as_uint(o[12]), false, false);
        const float l = __uint_as_float(sw[1]);
        const float il = __builtin_amdgcn_rcpf(l);
#pragma unroll
        for (int i = 0; i < 16; ++i) o[i] *= il;
        if (h) { o[12] = 0.f; o[13] = 0.f; o[14] = 0.f; o[15] = 0.f; }
        const unsigned off = WS_O + (tile * 16 + head) * 2048 + lane * 16;
        st16(rws, off, pack8(o, 0)); st16(rws, off + 1024, pack8(o, 1));
    }
    STAMP(10);
    if (!p3) {
        VM0_BAR();
        if (tid == 0) st_flag(P.ws + WS_FO + (qb * 16 + head) * 64, tag);
        if (b == 255 && w == 0) {
#pragma unroll 1
            for (int i = 0; i < 4; ++i) poll_flags(P.ws + WS_FQKV + (i * 64 + lane) * 64, true, tag);
            if (lane == 0) st_flag(P.ws + WS_EPOCH, tag);
        }
        return;
    }
    const unsigned q4 = (lane & 15) >> 2, trb = (4 * (lane >> 5) + q4) * 128 + ((lane >> 4) & 1) * 32 + (lane & 3) * 8 + q3 * 12 * 1024;
    const unsigned tro0 = trb + ((q4 >> 1) & 1) * 64, tro1 = trb + (((q4 >> 1) & 1) ^ 1) * 64;
    const unsigned tok = tile3 * 32 + l31, n0 = cg3 * 64 + q3 * 16;
    u32x2 xr[2], br[2];
#pragma unroll
    for (int gq = 0; gq < 2; ++gq) {
        const unsigned n = n0 + 8 * gq + 4 * h;
        xr[gq] = *(const u32x2*)((const char*)P.x + (tok * DM + n) * 2); br[gq] = *(const u32x2*)((const char*)P.b2 + n * 2);
    }
    f32x16 acc0 = {}, acc1 = {};
    u32x4 ao[8];
    static_for<6>([&](auto SC) {
        constexpr int sb = decltype(SC)::value;
        LAS char* wp0 = opaque(lds + p3_region(sb) + tro0); LAS char* wp1 = opaque(lds + p3_region(sb) + tro1);
        u32x4 w0[3], w1[3];
#pragma unroll
        for (int i = 0; i < 3; ++i) { w0[i] = tr_frag<1024>(wp0 + i * 2048); w1[i] = tr_frag<1024>(wp1 + i * 2048); }
        if (sb == 3) {
#pragma unroll
            for (int j = 0; j < 8; ++j) ao[j] = ld16(rws, lane * 16 + (j & 3) * 1024, WS_O + (tile3 * 32 + 8 * q3) * 1024 + (j >> 2) * 4096);
        }
        SBAR();
        static_for<6>([&](auto IC_) {
            constexpr int i = decltype(IC_)::value, j = 6 * sb + i;
            const u32x4 a = j < 28 ? af[j < 28 ? j : 0] : ao[j >= 28 ? j - 28 : 0];
            acc0 = mfma16(w0[i % 3], a, acc0);
            if constexpr (sb + 2 < 6) p3_dma<(sb + 2 < 6 ? sb + 2 : 0), i>(W3);
            acc1 = mfma16(w1[i % 3], a, acc1);
            if (i + 3 < 6) { w0[i % 3] = tr_frag<1024>(wp0 + (i + 3) * 2048); w1[i % 3] = tr_frag<1024>(wp1 + (i + 3) * 2048); }
            SBAR();
        });
        if (sb == 2 && w == 0) poll_flags(P.ws + WS_FO + ((tb3 >> 1) * 16 + (lane & 15)) * 64, lane < 16, tag);
        if (sb < 4) asm volatile("s_waitcnt vmcnt(6) lgkmcnt(0)\n\ts_barrier" ::: "memory");
        else if (sb == 4) VM0_BAR();
        if (sb == 0 && tid == 0) st_flag(P.ws + WS_FO + (qb * 16 + head) * 64, tag);
        if (sb == 1) asm volatile("" ::"v"(tch0), "v"(tch1));
        if (sb == 2) STAMP(11);
        if (sb == 4) STAMP(12);
    });
    BAR();
    STAMP(13);
    LAS float* rx = (LAS float*)lds;
    float v[8];
    auto scatter = [&](auto QC_) {
        constexpr int Q = decltype(QC_)::value;
#pragma unroll
        for (int d = 0; d < 4; ++d) {
            if (d != Q) {
#pragma unroll
                for (int i = 0; i < 8; ++i) rx[(((tt3 * 4 + d) * 4 + Q) * 8 + i) * 64 + lane] = (d >> 1) ? acc1[8 * (d & 1) + i] : acc0[8 * (d & 1) + i];
            } else {
#pragma unroll
                for (int i = 0; i < 8; ++i) v[i] = (d >> 1) ? acc1[8 * (d & 1) + i] : acc0[8 * (d & 1) + i];
            }
        }
    };
    if (q3 == 0) scatter(IC<0>{}); else if (q3 == 1) scatter(IC<1>{}); else if (q3 == 2) scatter(IC<2>{}); else scatter(IC<3>{});
    BAR();
#pragma unroll
    for (int s = 1; s < 4; ++s) {
        const unsigned sq = (q3 + s) & 3;
#pragma unroll
        for (int i = 0; i < 8; ++i) v[i] += rx[(((tt3 * 4 + q3) * 4 + sq) * 8 + i) * 64 + lane];
    }
    u32x2 pk[2];
#pragma unroll
    for (int gq = 0; gq < 2; ++gq) {
        const float y0 = v[4 * gq] + __uint_as_float(xr[gq][0] << 16) + __uint_as_float(br[gq][0] << 16), y1 = v[4 * gq + 1] + __uint_as_float(xr[gq][0] & 0xffff0000u) + __uint_as_float(br[gq][0] & 0xffff0000u);
        const float y2 = v[4 * gq + 2] + __uint_as_float(xr[gq][1] << 16) + __uint_as_float(br[gq][1] << 16), y3 = v[4 * gq + 3] + __uint_as_float(xr[gq][1] & 0xffff0000u) + __uint_as_float(br[gq][1] & 0xffff0000u);
        pk[gq] = u32x2{cvtpk(y0, y1), cvtpk(y2, y3)};
    }
    {
        auto s0 = __builtin_amdgcn_permlane32_swap(pk[0][0], pk[1][0], false, false), s1 = __builtin_amdgcn_permlane32_swap(pk[0][1], pk[1][1], false, false);
        const u32x4 o4 = {s0[0], s1[0], s0[1], s1[1]};
        __builtin_amdgcn_raw_buffer_store_b128(o4, mk_rsrc(P.out, SEQ * DM * 2), (tok * DM + n0 + 8 * h) * 2, 0, OUT_AUX);
    }
    STAMP(14);
    asm volatile("fk_code_end:");
}

__global__ __launch_bounds__(512, 2) void fused_kernel(const void* x, const void* wqkv, const void* wo, const void* w1, const void* b1, const void* w2, const void* b2, void* out, char* ws) {
    extern __shared__ __attribute__((aligned(16))) char lds_raw[];
    LAS char* lds = (LAS char*)lds_raw;
    const Ptrs P{x, wqkv, wo, w1, b1, w2, b2, out, ws};
    STAMP(0);
    u32x4 qf0, qf1; float kn2; P3W W3;
    const u64 tag = phase1(P, lds, blockIdx.x, qf0, qf1, kn2, W3);
    phase23(P, lds, blockIdx.x, tag, qf0, qf1, kn2, W3);
}

extern "C" void kernel_launch(void* const* d_in, const int* in_sizes, int n_in, void* d_out, int out_size, void* d_ws, size_t ws_size, hipStream_t stream) {
    (void)in_sizes; (void)n_in; (void)out_size; (void)ws_size;
    (void)hipFuncSetAttribute((const void*)fused_kernel, hipFuncAttributeMaxDynamicSharedMemorySize, LDS_BYTES);
    fused_kernel<<<256, 512, LDS_BYTES, stream>>>(d_in[0], d_in[1], d_in[2], d_in[4], d_in[5], d_in[6], d_in[7], d_out, (char*)d_ws);
}
```

```cpp
#include <hip/hip_runtime.h>
#include <hip/hip_bf16.h>
#include <stdint.h>
#include <type_traits>
#include <utility>

#define DI __device__ __forceinline__
#define LAS __attribute__((address_space(3)))
#define GAS __attribute__((address_space(1)))
typedef float f32x16 __attribute__((ext_vector_type(16)));
typedef float f32x4 __attribute__((ext_vector_type(4)));
typedef float f32x2 __attribute__((ext_vector_type(2)));
typedef __bf16 bf16x8 __attribute__((ext_vector_type(8)));
typedef __bf16 bf16x2 __attribute__((ext_vector_type(2)));
typedef unsigned u32x4 __attribute__((ext_vector_type(4)));
typedef unsigned u32x2 __attribute__((ext_vector_type(2)));
typedef short s16x4 __attribute__((ext_vector_type(4)));
typedef unsigned long long u64;
typedef __amdgpu_buffer_rsrc_t rsrc_t;

constexpr int SEQ = 2048, DM = 448, NH = 16, DH = 28, FF = 1792;
constexpr unsigned WS_EPOCH = 0, WS_FQKV = 4096, WS_FH = WS_FQKV + 256 * 64, WS_FO = WS_FH + 256 * 64;
constexpr unsigned WS_KN2 = 512u << 10;
constexpr unsigned WS_Q = 1u << 20, WS_K = 3u << 20, WS_V = 5u << 20, WS_O = 7u << 20, WS_H = 9u << 20;
constexpr unsigned WS_BYTES = 17u << 20;
constexpr float C2 = 0.27264234f;
constexpr float LN_EPS = 1e-5f;
constexpr int AUX_SC1 = 16;
constexpr unsigned LDS_BYTES = 160 * 1024;
constexpr unsigned SPIN_MAX = 200000;
constexpr float SAFE_BOUND_V = 40.f;
constexpr unsigned OOB = 0x7ffffff0u;

DI rsrc_t mk_rsrc(const void* p, unsigned bytes) { return __builtin_amdgcn_make_buffer_rsrc((void*)p, (short)0, (int)bytes, 0x00020000); }
DI unsigned rfl(unsigned x) { return __builtin_amdgcn_readfirstlane(x); }
DI unsigned rfl_pad(unsigned x) { unsigned r = rfl(x); asm volatile("s_nop 4" : "+s"(r)); return r; }
DI void dma16(rsrc_t rs, unsigned voff, unsigned soff, unsigned ldsaddr) {
    asm volatile("s_mov_b32 m0, %0\n\ts_nop 0\n\tbuffer_load_dwordx4 %1, %2, %3 offen lds" ::"s"(ldsaddr), "v"(voff), "s"(rs), "s"(soff) : "memory");
}
DI void dma16_sc1(rsrc_t rs, unsigned voff, unsigned soff, unsigned ldsaddr) {
    asm volatile("s_mov_b32 m0, %0\n\ts_nop 0\n\tbuffer_load_dwordx4 %1, %2, %3 offen sc1 lds" ::"s"(ldsaddr), "v"(voff), "s"(rs), "s"(soff) : "memory");
}
#define WAIT_VM0() asm volatile("s_waitcnt vmcnt(0)" ::: "memory")
#define BAR() asm volatile("s_waitcnt lgkmcnt(0)\n\ts_barrier" ::: "memory")
#define VM0_BAR() asm volatile("s_waitcnt vmcnt(0) lgkmcnt(0)\n\ts_barrier" ::: "memory")
#define VMCNT(n) asm volatile("s_waitcnt vmcnt(%0)" ::"n"(n) : "memory")
#define VMCNT_BAR(n) asm volatile("s_waitcnt vmcnt(%0) lgkmcnt(0)\n\ts_barrier" ::"n"(n) : "memory")
#define SBAR() __builtin_amdgcn_sched_barrier(0)
#define PIN(x) asm volatile("" : "+v"(x))

template <int V> using IC = std::integral_constant<int, V>;
template <int... Is, class F> DI void static_for_(std::integer_sequence<int, Is...>, F&& f) { (f(IC<Is>{}), ...); }
template <int N, class F> DI void static_for(F&& f) { static_for_(std::make_integer_sequence<int, N>{}, f); }

DI f32x16 mfma16(u32x4 a, u32x4 b, f32x16 c) { return __builtin_amdgcn_mfma_f32_32x32x16_bf16(__builtin_bit_cast(bf16x8, a), __builtin_bit_cast(bf16x8, b), c, 0, 0, 0); }
DI f32x16 mfma_f32(float a, float b, f32x16 c) { return __builtin_amdgcn_mfma_f32_32x32x2f32(a, b, c, 0, 0, 0); }
DI unsigned cvtpk(float lo, float hi) { f32x2 v = {lo, hi}; return __builtin_bit_cast(unsigned, __builtin_convertvector(v, bf16x2)); }
DI float dot2(unsigned a, unsigned b, float c) { return __builtin_amdgcn_fdot2_f32_bf16(__builtin_bit_cast(bf16x2, a), __builtin_bit_cast(bf16x2, b), c, false); }
DI float swap_add(float x) { auto r = __builtin_amdgcn_permlane32_swap(__float_as_uint(x), __float_as_uint(x), false, false); return __uint_as_float(r[0]) + __uint_as_float(r[1]); }
DI float swap_max(float x) { auto r = __builtin_amdgcn_permlane32_swap(__float_as_uint(x), __float_as_uint(x), false, false); return fmaxf(__uint_as_float(r[0]), __uint_as_float(r[1])); }
template <int CTRL> DI float dpp_max(float x) { return fmaxf(x, __uint_as_float(__builtin_amdgcn_update_dpp(0u, __float_as_uint(x), CTRL, 0xf, 0xf, true))); }
DI float max32(float x) {
    x = dpp_max<0x128>(x); x = dpp_max<0x124>(x); x = dpp_max<0x122>(x); x = dpp_max<0x121>(x);
    auto sw = __builtin_amdgcn_permlane16_swap(__float_as_uint(x), __float_as_uint(x), false, false);
    return fmaxf(__uint_as_float(sw[0]), __uint_as_float(sw[1]));
}
template <int D2> DI u32x4 tr_frag(LAS char* p) {
    s16x4 lo = __builtin_amdgcn_ds_read_tr16_b64_v4i16((LAS s16x4*)p), hi = __builtin_amdgcn_ds_read_tr16_b64_v4i16((LAS s16x4*)(p + D2));
    u32x2 a = __builtin_bit_cast(u32x2, lo), b = __builtin_bit_cast(u32x2, hi);
    return u32x4{a[0], a[1], b[0], b[1]};
}
DI u32x4 pack8(const f32x16& v, int s) { return u32x4{cvtpk(v[8 * s], v[8 * s + 1]), cvtpk(v[8 * s + 2], v[8 * s + 3]), cvtpk(v[8 * s + 4], v[8 * s + 5]), cvtpk(v[8 * s + 6], v[8 * s + 7])}; }
DI void st16(rsrc_t rs, unsigned off, u32x4 v) { __builtin_amdgcn_raw_buffer_store_b128(v, rs, off, 0, AUX_SC1); }
DI u32x4 ld16(rsrc_t rs, unsigned voff, unsigned soff) { return __builtin_amdgcn_raw_buffer_load_b128(rs, voff, soff, AUX_SC1); }
DI float bf2f(unsigned short b) { return __uint_as_float((unsigned)b << 16); }
DI u64 ld_flag(const char* p) { return __hip_atomic_load((GAS const u64*)p, __ATOMIC_RELAXED, __HIP_MEMORY_SCOPE_AGENT); }
DI void st_flag(char* p, u64 v) { __hip_atomic_store((GAS u64*)p, v, __ATOMIC_RELAXED, __HIP_MEMORY_SCOPE_AGENT); }
DI void poll_flags(const char* p, bool use, u64 tag) {
    for (unsigned n = 0; n < SPIN_MAX; ++n) {
        const u64 v = use ? ld_flag(p) : tag;
        if (__all(v == tag)) break;
        __builtin_amdgcn_s_sleep(1);
    }
    asm volatile("" ::: "memory");
}
DI void ld_flag_async(u64& v, const char* p) { asm volatile("global_load_dwordx2 %0, %1, off sc1" : "=v"(v) : "v"(p) : "memory"); }
template <class T> DI T* opaque(T* p) { asm volatile("" : "+v"(p)); return p; }
#define LDF(p) (*(LAS u32x4*)(p))

struct Ptrs { const void* x; const void* wqkv; const void* wo; const void* w1; const void* b1; const void* w2; const void* b2; void* out; char* ws; };

constexpr unsigned L_STAT = 155648, L_CS = 156672;
DI void sum8(const u32x4& f, float& a, float& b) {
    float t0, t1, t2, t3;
    asm volatile("v_lshlrev_b32 %2, 16, %6\n\tv_and_b32 %3, 0xffff0000, %6\n\tv_lshlrev_b32 %4, 16, %7\n\tv_and_b32 %5, 0xffff0000, %7\n\t"
                 "v_add_f32 %0, %0, %2\n\tv_add_f32 %1, %1, %3\n\tv_add_f32 %0, %0, %4\n\tv_add_f32 %1, %1, %5\n\t"
                 "v_lshlrev_b32 %2, 16, %8\n\tv_and_b32 %3, 0xffff0000, %8\n\tv_lshlrev_b32 %4, 16, %9\n\tv_and_b32 %5, 0xffff0000, %9\n\t"
                 "v_add_f32 %0, %0, %2\n\tv_add_f32 %1, %1, %3\n\tv_add_f32 %0, %0, %4\n\tv_add_f32 %1, %1, %5"
                 : "+v"(a), "+v"(b), "=&v"(t0), "=&v"(t1), "=&v"(t2), "=&v"(t3) : "v"(f[0]), "v"(f[1]), "v"(f[2]), "v"(f[3]));
}
struct P1Dma { rsrc_t rqkv, rw1, rw2, rwo; unsigned vq0, vq1, vq2, vf, qsoff, qdst, fsoff, fdst, tsoff, tdst, usoff, udst, tdummy, vt2, vto; };

struct QkvAddr { LAS char* qa[4]; LAS char* qa2[4]; LAS char* xu[4]; };
template <int HALF, bool SWA, bool SWB>
DI void p1_qkv_pass(const Ptrs& P, const P1Dma& D, u32x4 (&xf)[28], const QkvAddr& A, bool is28, f32x16& accA, f32x16& accB, float& csA, float& csB) {
    constexpr int PF = 3;
    u32x4 wa[PF], wb[PF];
    float a0 = 0.f, a1 = 0.f, b0 = 0.f, b1 = 0.f;
    const unsigned ones = 0x3F803F80u;
    accA = f32x16{}; accB = f32x16{};
    auto rd = [&](auto SC) {
        constexpr int s = decltype(SC)::value; constexpr int o = s < 12 ? (s & ~3) * 3072 : ((s & ~3) - 12) * 3072;
        wa[s % PF] = tr_frag<768>((s < 12 ? A.qa[s & 3] : A.qa2[s & 3]) + o); if (HALF == 0) wb[s % PF] = tr_frag<768>((s < 12 ? A.qa[s & 3] : A.qa2[s & 3]) + o + 56);
    };
    rd(IC<0>{}); rd(IC<1>{}); rd(IC<2>{});
    SBAR();
    static_for<28>([&](auto SC) {
        constexpr int s = decltype(SC)::value;
        u32x4 ca = wa[s % PF]; const u32x4 cb = wb[s % PF];
        if (HALF == 1) { ca[0] = is28 ? ones : ca[0]; ca[1] = is28 ? ones : ca[1]; ca[2] = is28 ? ones : ca[2]; ca[3] = is28 ? ones : ca[3]; }
        accA = SWA ? mfma16(xf[s], ca, accA) : mfma16(ca, xf[s], accA);
        if constexpr (s < 6) dma16(D.rqkv, s % 3 == 0 ? D.vq0 : s % 3 == 1 ? D.vq1 : D.vq2, D.qsoff + (s / 3) * (16 * 2688), D.qdst + s * 1024);
        else if constexpr (s >= 12 && s < 20) dma16(D.rw1, D.vf, D.fsoff + (s - 12) * (4 * 3584), D.fdst + (s - 12) * 1024);
        else if constexpr (s >= 20 && s < 24) dma16(D.rw1, D.vf, D.tsoff + (s - 20) * (4 * 3584), D.tdst + (s - 20) * 1024);
        if (HALF == 0) accB = SWB ? mfma16(xf[s], cb, accB) : mfma16(cb, xf[s], accB);
        else accB = mfma16(xf[s], xf[s], accB);
        if constexpr (s + PF < 28 && !(s + PF >= 12 && s <= 11)) rd(IC<(s + PF < 28 ? s + PF : 0)>{});
        if constexpr (s >= 4 && s < 8) {
#pragma unroll
            for (int i = 0; i < 3; ++i) { constexpr int dummy = 0; (void)dummy; const int j = 3 * (s - 4) + i; xf[16 + j] = LDF(A.xu[j & 3] + 128 * (j >> 2)); }
        }
        if constexpr ((s & 3) == 0) { sum8(ca, a0, a1); if (HALF == 0) sum8(cb, b0, b1); }
        SBAR();
        if constexpr (s == 24) {
            asm volatile("s_mov_b32 m0, %0\n\ts_nop 0\n\tbuffer_load_dword %1, %2, 0 offen lds\n\tbuffer_load_dword %3, %4, 0 offen lds" ::"s"(D.tdummy), "v"(D.vt2), "s"(D.rw2), "v"(D.vto), "s"(D.rwo) : "memory");
        }
        if constexpr (s == 3) { VMCNT_BAR(6); SBAR(); }
        if constexpr (s == 11) {
            VMCNT_BAR(0);
            rd(IC<12>{}); rd(IC<13>{}); rd(IC<14>{});
            SBAR();
        }
    });
    csA = a0 + a1; csB = b0 + b1;
}
DI void p1_ffn_pass(const Ptrs& P, LAS char* lds, const P1Dma& D, const u32x4 (&xf)[28], unsigned tt, unsigned off, f32x16& accA, f32x16& accB, float& csA, float& csB,
                    char* flag, u64 tag, const char* qflag, u64& fv) {
    LAS char* q[4]; LAS char* q2[4]; LAS char* p[4]; LAS char* p2[4];
#pragma unroll
    for (int u = 0; u < 4; ++u) {
        const unsigned ro = off + ((u + tt) & 3) * 4096;
        q[u] = opaque(lds + 49152 + ro); q2[u] = opaque(lds + 114688 + ro); p[u] = opaque(lds + 49152 + (ro ^ 64)); p2[u] = opaque(lds + 114688 + (ro ^ 64));
    }
    constexpr int PF = 3;
    u32x4 wa[PF], wb[PF];
    float a0 = 0.f, a1 = 0.f, b0 = 0.f, b1 = 0.f;
    accA = f32x16{}; accB = f32x16{};
    auto rd = [&](auto SC) {
        constexpr int s = decltype(SC)::value;
        if constexpr (s < 16) { wa[s % PF] = tr_frag<1024>(q[s & 3] + (s & ~3) * 4096); wb[s % PF] = tr_frag<1024>(p[s & 3] + (s & ~3) * 4096); }
        else if constexpr (s < 24) { wa[s % PF] = tr_frag<1024>(q2[s & 3] + ((s & ~3) - 16) * 4096); wb[s % PF] = tr_frag<1024>(p2[s & 3] + ((s & ~3) - 16) * 4096); }
        else { wa[s % PF] = tr_frag<1024>(q[s & 3] - 49152); wb[s % PF] = tr_frag<1024>(p[s & 3] - 49152); }
    };
    rd(IC<0>{}); rd(IC<1>{}); rd(IC<2>{});
    SBAR();
    static_for<28>([&](auto SC) {
        constexpr int s = decltype(SC)::value;
        const u32x4 ca = wa[s % PF], cb = wb[s % PF];
        accA = mfma16(ca, xf[s], accA);
        accB = mfma16(cb, xf[s], accB);
        if constexpr (s + PF < 28) rd(IC<(s + PF < 28 ? s + PF : 0)>{});
        if constexpr ((s & 3) == 0) sum8(ca, a0, a1);
        if constexpr ((s & 3) == 0) sum8(cb, b0, b1);
        if constexpr (s == 26) fv = ld_flag(qflag);
        SBAR();
        if constexpr (s == 12) {
            VMCNT_BAR(10);
            SBAR();
        }
        if constexpr (s == 18) {
            VMCNT_BAR(0);
            if (threadIdx.x == 0) st_flag(flag, tag);
            SBAR();
        }
    });
    csA = a0 + a1; csB = b0 + b1;
}
DI float p1_cs(LAS float* csb, unsigned l31) {
    float v[8];
#pragma unroll
    for (int i = 0; i < 8; ++i) v[i] = csb[i * 32 + l31];
    return ((v[0] + v[1]) + (v[2] + v[3])) + ((v[4] + v[5]) + (v[6] + v[7]));
}
struct P1Ctx { rsrc_t rws; char* ws; unsigned lane, l31, h, tile; float mu, sig, rstd; };
DI void p1_gelu(const P1Ctx& C, f32x16 acc, float cs, float bias, u32x4& pk0, u32x4& pk1) {
    acc = mfma_f32(C.h ? bias : cs, C.h ? C.sig : -C.mu, acc);
    const float k0 = 2.302208198f * C.rstd, k1 = 0.1029432397f * C.rstd * C.rstd * C.rstd;
#pragma unroll
    for (int i = 0; i < 16; i += 2) {
        const f32x2 a = f32x2{acc[i], acc[i + 1]}, z = a * __builtin_elementwise_fma(a * a, f32x2{k1, k1}, f32x2{k0, k0});
        const f32x2 d = __builtin_elementwise_fma(f32x2{__builtin_amdgcn_exp2f(-z[0]), __builtin_amdgcn_exp2f(-z[1])}, f32x2{C.sig, C.sig}, f32x2{C.sig, C.sig});
        const f32x2 y = a * f32x2{__builtin_amdgcn_rcpf(d[0]), __builtin_amdgcn_rcpf(d[1])};
        acc[i] = y[0]; acc[i + 1] = y[1];
    }
    pk0 = pack8(acc, 0); pk1 = pack8(acc, 1);
}
DI void p1_store_h(const P1Ctx& C, unsigned ft, u32x4 pk0, u32x4 pk1) {
    const unsigned off = WS_H + (C.tile * 112 + 2 * ft) * 1024 + C.lane * 16;
    st16(C.rws, off, pk0); st16(C.rws, off + 1024, pk1);
}
DI void p1_epi_qkv(const P1Ctx& C, f32x16 acc, float cs, unsigned type, unsigned head) {
    const unsigned h = C.h, l31 = C.l31;
    const bool isv = type == 2;
    const float m0 = h ? 0.f : -C.mu, c0 = h ? 0.f : cs, r0 = h ? 0.f : (type == 0 ? C.rstd * C2 : C.rstd), k0 = (h || l31 >= 28) ? 0.f : 1.f;
    acc = mfma_f32(isv ? m0 : c0, isv ? c0 : m0, acc);
    f32x16 R = {}; R = mfma_f32(isv ? r0 : k0, isv ? k0 : r0, R);
    const float pv = (isv && l31 == 28) ? 1.f : 0.f, p12 = isv ? pv : ((type == 1 && h) ? 1.f : 0.f);
    f32x2 n2 = {0.f, 0.f};
#pragma unroll
    for (int i = 0; i < 16; i += 2) {
        const f32x2 t = __builtin_elementwise_fma(f32x2{acc[i], acc[i + 1]}, f32x2{R[i], R[i + 1]}, f32x2{i == 12 ? p12 : pv, pv});
        acc[i] = t[0]; acc[i + 1] = t[1];
        n2 = __builtin_elementwise_fma(t, t, n2);
    }
    const unsigned off = WS_Q + type * (2u << 20) + (head * 64 + C.tile) * 2048 + C.lane * 16;
    st16(C.rws, off, pack8(acc, 0)); st16(C.rws, off + 1024, pack8(acc, 1));
    const float kn = max32(swap_add(n2[0] + n2[1]));
    __builtin_amdgcn_raw_buffer_store_b32(__float_as_uint(kn), C.rws, (type == 1 && C.lane == 0) ? WS_KN2 + (head * 64 + C.tile) * 4 : OOB, 0, AUX_SC1);
}

DI u64 make_tag(u64 e) {
    e = ((u64)rfl((unsigned)(e >> 32)) << 32) | rfl((unsigned)e);
    u64 tag = (e + 0x9E3779B97F4A7C15ull); tag = (tag ^ (tag >> 30)) * 0xBF58476D1CE4E5B9ull; tag = (tag ^ (tag >> 27)) * 0x94D049BB133111EBull; tag ^= tag >> 31;
    return tag;
}
constexpr unsigned p3_region(int sb) { return (sb % 3) * 49152u; }
constexpr unsigned RING = 98304;
constexpr unsigned ring_off(int c) { return c % 4 == 2 ? 0u : c % 4 == 0 ? 16384u : c % 4 == 1 ? 32768u : 49152u; }
struct P3W { rsrc_t rw2, rwo; unsigned vw, vo0, vo1, soff, dst, hd0, vt2, vto; };
template <int SB, int I> DI void p3_dma(const P3W& W) {
    constexpr int j = 6 * SB + I;
    if constexpr (j < 28) dma16(W.rw2, W.vw, W.soff + j * (16 * DM * 2), W.dst + p3_region(SB) + I * 2048);
    else {
        dma16(W.rwo, ((j - 28) & 1) ? W.vo1 : W.vo0, W.hd0 + ((j - 28) >> 1) * (28 * DM * 2), W.dst + p3_region(SB) + I * 2048);
    }
}
DI P3W make_p3w(const Ptrs& P, unsigned lds0, int b, unsigned w, unsigned lane) {
    const unsigned r = b >> 3, cg3 = r < 28 ? r >> 2 : 0;
    P3W W3;
    const unsigned vcol = cg3 * 128 + (((lane >> 2) & 1) ^ ((lane >> 4) & 1)) * 64 + (lane & 3) * 16, rw = (w & 1) * 8 + (lane >> 3);
    W3.rw2 = mk_rsrc(P.w2, FF * DM * 2); W3.rwo = mk_rsrc(P.wo, DM * DM * 2);
    W3.vw = rw * (DM * 2) + vcol; W3.vo0 = W3.vw; W3.vo1 = W3.vw + 16 * (DM * 2);
    W3.soff = rfl_pad(28 * (w >> 1) * (16 * DM * 2)); W3.hd0 = rfl_pad(4 * (w >> 1) * (28 * DM * 2));
    W3.dst = rfl_pad(lds0 + (w >> 1) * 6 * 2048 + (w & 1) * 1024);
    {
        const unsigned L = w * 64 + lane, j = 12 + (L >> 4), row = L & 15, qq = r & 3;
        W3.vt2 = w < 4 ? ((28 * qq + j) * 16 + row) * (DM * 2) + cg3 * 128 : OOB;
        W3.vto = (w == 4 || w == 5) ? (28 * (4 * qq + ((j - 28) >> 1)) + 16 * ((j - 28) & 1) + row) * (DM * 2) + cg3 * 128 : OOB;
    }
    return W3;
}
DI void kv_first(const Ptrs& P, LAS char* lds, int b, unsigned w, unsigned lane, u32x4& qf0, u32x4& qf1, float& kn2) {
    const unsigned g = b & 7, r = b >> 3, head = 2 * g + (r >> 4), tile = (r & 15) * 4 + (w & 3);
    const rsrc_t rws = mk_rsrc(P.ws, WS_BYTES);
    const unsigned src = rfl_pad(((w & 1) ? WS_V : WS_K) + (head * 64 + (w >> 2) * 32 + ((w >> 1) & 1)) * 2048), wdst = rfl_pad((unsigned)(size_t)lds + 98304 + w * 2048);
    dma16_sc1(rws, lane * 16, src, wdst + 16384); dma16_sc1(rws, lane * 16 + 1024, src, wdst + 16384 + 1024);
    dma16_sc1(rws, lane * 16, src + 4096, wdst + 32768); dma16_sc1(rws, lane * 16 + 1024, src + 4096, wdst + 32768 + 1024);
    const unsigned qs = rfl_pad(WS_Q + (head * 64 + tile) * 2048), ks = rfl_pad(WS_KN2 + head * 256);
    asm volatile("buffer_load_dwordx4 %0, %3, %5, %6 offen sc1\n\tbuffer_load_dwordx4 %1, %3, %5, %6 offen offset:1024 sc1\n\tbuffer_load_dword %2, %4, %5, %7 offen sc1"
                 : "=&v"(qf0), "=&v"(qf1), "=&v"(kn2) : "v"(lane * 16), "v"(lane * 4), "s"(rws), "s"(qs), "s"(ks) : "memory");
}
DI u64 phase1(const Ptrs& P, LAS char* lds, int b, u32x4& qf0, u32x4& qf1, float& kn2, P3W& W3) {
    const unsigned tid = threadIdx.x, lane = tid & 63, l31 = lane & 31, h = lane >> 5;
    const unsigned w = rfl(tid >> 6), tt = w & 3, half = w >> 2;
    const unsigned g = b & 7, r = b >> 3, tb = 4 * (g >> 1) + (r & 3), cg = 8 * (g & 1) + (r >> 2);
    const unsigned lds0 = (unsigned)(size_t)lds;
    const rsrc_t rx = mk_rsrc(P.x, SEQ * DM * 2);
    P1Dma D;
    D.rqkv = mk_rsrc(P.wqkv, DM * 3 * DM * 2);
    const unsigned cb = 168 * cg, cb16 = cb & ~15u, dl = cb & 15u;
    {
        const unsigned rowa = 2 * w + h;
        const unsigned va = rowa * 896 + ((l31 ^ (rowa & 15)) << 4), sa = tb * 128 * 896;
#pragma unroll
        for (unsigned i = 0; i < 8; ++i) dma16(rx, va, sa + i * (16 * 896), lds0 + (w + 8 * i) * 1024);
    }
    {
        unsigned long long pc; unsigned len;
        asm volatile("s_getpc_b64 %0\n\tfk_code_here:\n\ts_mov_b32 %1, fk_code_end-fk_code_here" : "=s"(pc), "=s"(len));
        const unsigned per = (len / 128 + 31) / 32, line = r * per + w * 64 + lane;
        const bool on = w * 64 + lane < per && line * 128 < len;
        const unsigned long long a = pc + (on ? line * 128 : 0);
        asm volatile("s_mov_b32 m0, %0\n\ts_nop 0\n\tglobal_load_lds_dword %1, off" ::"s"(lds0 + 151552 + w * 256), "v"(a) : "memory");
    }
    {
        auto vq = [&](unsigned p) { const unsigned row = p / 12, ch = p - row * 12; return ch < 11 ? row * 2688 + cb16 + ch * 16 : OOB; };
        D.vq0 = vq(lane); D.vq1 = vq(64 + lane); D.vq2 = vq(128 + lane);
    }
    const unsigned ntile = ((cg ^ (cg >> 1)) & 1) ? 3 : 4, fstart = 7 * (cg >> 1) + ((cg & 1) ? (((cg >> 1) & 1) ? 3 : 4) : 0);
    D.rw1 = mk_rsrc(P.w1, DM * FF * 2);
    { const unsigned rq = lane >> 4, sl = ((lane >> 2) & 3) ^ rq; D.vf = sl < ntile ? rq * 3584 + (fstart + sl) * 64 + (lane & 3) * 16 : OOB; }
    D.qsoff = (12 + 2 * w) * (16 * 2688); D.qdst = lds0 + 2 * w * 3072;
    D.fsoff = 2 * w * (16 * 3584); D.fdst = lds0 + 49152 + 2 * w * 4096;
    D.tsoff = (256 + 16 * w) * 3584; D.tdst = lds0 + 114688 + w * 4096;
    D.usoff = (384 + 8 * w) * 3584; D.udst = lds0 + 2 * w * 1024;
    D.rw2 = mk_rsrc(P.w2, FF * DM * 2); D.rwo = mk_rsrc(P.wo, DM * DM * 2); D.tdummy = lds0 + 151552 + w * 256;
    { const unsigned i = w * 8 + (lane & 7); D.vt2 = (lane < 8 && i < 49) ? (b * 49 + i) * 128 : OOB; D.vto = (lane < 8 && i < 13) ? (b * 13 + i) * 128 : OOB; }
    const u64 eraw = ld_flag(P.ws + WS_EPOCH);
    const unsigned short b1a = ((const unsigned short*)P.b1)[(fstart + 2 * half) * 32 + l31];
    const unsigned short b1b = ((const unsigned short*)P.b1)[(2 * half + 1 < ntile ? fstart + 2 * half + 1 : fstart) * 32 + l31];
    {
        const unsigned so = w * (16 * 2688), ds = lds0 + 114688 + w * 3072;
        if (w < 4) { dma16(D.rqkv, D.vq0, so, ds); dma16(D.rqkv, D.vq1, so, ds + 1024); dma16(D.rqkv, D.vq2, so, ds + 2048); }
        dma16(D.rqkv, D.vq0, so + 4 * (16 * 2688), ds + 4 * 3072); dma16(D.rqkv, D.vq1, so + 4 * (16 * 2688), ds + 4 * 3072 + 1024); dma16(D.rqkv, D.vq2, so + 4 * (16 * 2688), ds + 4 * 3072 + 2048);
    }
#pragma unroll
    for (unsigned i = 0; i < 6; ++i) {
        const unsigned p = 64 * i + lane, rr = p / 24, c = p - rr * 24, row = 16 * w + rr;
        dma16(rx, row * 896 + 512 + ((c ^ ((row >> 1) & 7)) << 4), tb * 128 * 896, lds0 + 65536 + (6 * w + i) * 1024);
    }
    const rsrc_t rws = mk_rsrc(P.ws, WS_BYTES);
    LAS float* stat = (LAS float*)(lds + L_STAT);
    LAS float* csb = (LAS float*)(lds + L_CS);
    P1Ctx C{rws, P.ws, lane, l31, h, tb * 4 + tt, 0.f, 0.f, 0.f};
    const unsigned G = lane >> 4, q4 = (lane & 15) >> 2;
    const unsigned trq = (8 * (G >> 1) + q4) * 192 + dl + (G & 1) * 32 + (lane & 3) * 8 + half * 112;
    const unsigned trf = (8 * (G >> 1) + q4) * 256 + (G & 1) * 32 + (lane & 3) * 8;
    f32x16 accA, accB; float csA, csB;
    const unsigned tA = 3 * cg + 2 * half, tB = 3 * cg + 1, tyA = tA >> 4, tyB = tB >> 4;
    QkvAddr QA;
    {
        LAS char* xb0 = lds + 65536 + (tt * 32 + l31) * 384; const unsigned gswb = (l31 >> 1) & 7;
#pragma unroll
        for (int u = 0; u < 4; ++u) {
            const unsigned ro = ((u + tt) & 3) * 3072;
            QA.qa[u] = opaque(lds + 114688 + trq + ro); QA.qa2[u] = opaque(lds + trq + ro);
            QA.xu[u] = opaque(xb0 + (((2 * ((u + tt) & 3) + h) ^ gswb) << 4));
        }
    }
    const bool m28 = l31 == 28;
    {
        const unsigned i5 = w * 5 + lane, i7 = w * 7 + lane, g5 = b * 37 + i5;
        const unsigned v5 = (lane < 5 && i5 < 37 && g5 < 9408) ? g5 * 128 : OOB, v7 = (lane < 7 && i7 < 49) ? (b * 49 + i7) * 128 : OOB;
        asm volatile("s_mov_b32 m0, %0\n\ts_nop 0\n\tbuffer_load_dword %1, %2, 0 offen lds\n\tbuffer_load_dword %3, %4, 0 offen lds" ::"s"(D.tdummy), "v"(v5), "s"(D.rqkv), "v"(v7), "s"(D.rw1) : "memory");
    }
    VMCNT_BAR(11);
    u32x4 xf[28];
    {
        LAS char* xa = lds + (tt * 32 + l31) * 512;
#pragma unroll
        for (int u = 0; u < 4; ++u) {
            const unsigned c = 2 * ((u + tt) & 3) + h;
            LAS char* x0 = xa + ((c ^ (l31 & 15)) << 4); LAS char* x1 = xa + (((c + 8) ^ (l31 & 15)) << 4);
            xf[u] = LDF(x0); xf[4 + u] = LDF(x1); xf[8 + u] = LDF(x0 + 256); xf[12 + u] = LDF(x1 + 256);
        }
    }
    const u64 tag = make_tag(eraw);
    VMCNT_BAR(8);
    if (half == 0) {
        if (tyA == 2) p1_qkv_pass<0, true, true>(P, D, xf, QA, m28, accA, accB, csA, csB);
        else if (tyB == 2) p1_qkv_pass<0, false, true>(P, D, xf, QA, m28, accA, accB, csA, csB);
        else p1_qkv_pass<0, false, false>(P, D, xf, QA, m28, accA, accB, csA, csB);
        csb[0 * 256 + tt * 64 + lane] = csA; csb[1 * 256 + tt * 64 + lane] = csB;
    } else {
        float a1;
        if (tyA == 2) {
            p1_qkv_pass<1, true, false>(P, D, xf, QA, m28, accA, accB, csA, csB);
            if (l31 == 28) {
#pragma unroll
                for (int i = 0; i < 16; ++i) stat[(tt * 32 + (i & 3) + 8 * (i >> 2) + 4 * h) * 2] = accA[i];
            }
            asm volatile("s_waitcnt lgkmcnt(0)" ::: "memory");
            a1 = stat[(tt * 32 + l31) * 2];
        } else {
            p1_qkv_pass<1, false, false>(P, D, xf, QA, m28, accA, accB, csA, csB);
            auto sw = __builtin_amdgcn_permlane32_swap(__float_as_uint(accA[12]), __float_as_uint(accA[12]), false, false);
            a1 = __uint_as_float(sw[1]);
        }
        csb[2 * 256 + tt * 64 + lane] = csA;
        const unsigned ri = (l31 & 3) + 4 * (l31 >> 3);
        float dg = 0.f;
#pragma unroll
        for (int i = 0; i < 16; ++i) dg = ri == (unsigned)i ? accB[i] : dg;
        const float a2 = swap_add(h == ((l31 >> 2) & 1) ? dg : 0.f);
        const float m = a1 * (1.f / DM), var = fmaxf(a2 * (1.f / DM) - m * m, 0.f) + LN_EPS;
        if (!h) *(LAS f32x2*)(stat + (tt * 32 + l31) * 2) = f32x2{m, __builtin_amdgcn_rsqf(var)};
    }
    VMCNT_BAR(6);
    dma16(D.rw1, D.vf, D.usoff, D.udst); dma16(D.rw1, D.vf, D.usoff + 4 * 3584, D.udst + 1024);
    { const f32x2 st = *(LAS f32x2*)(stat + (tt * 32 + l31) * 2); C.mu = st[0]; C.rstd = st[1]; C.sig = __builtin_amdgcn_rcpf(st[1]); }
    {
        const float c0 = p1_cs(csb + 2 * half * 256, l31), c1 = p1_cs(csb + 256, l31);
        p1_epi_qkv(C, accA, c0, tyA, tA & 15);
        if (half == 0) p1_epi_qkv(C, accB, c1, tyB, tB & 15);
        else asm volatile("buffer_store_dword %0, %0, %1, 0 offen sc1\n\tbuffer_store_dword %0, %0, %1, 0 offen sc1\n\tbuffer_store_dword %0, %0, %1, 0 offen sc1" ::"v"(OOB), "s"(rws) : "memory");
    }
    const unsigned head2 = 2 * g + (r >> 4), qb2 = r & 15;
    const char* qflag;
    { const unsigned cq = head2 / 3, ck = (16 + head2) / 3, cv = (32 + head2) / 3;
      const unsigned idx = lane < 16 ? ck * 16 + lane : lane < 32 ? cv * 16 + (lane - 16) : cq * 16 + qb2; qflag = P.ws + WS_FQKV + (lane <= 32 ? idx : 0) * 64; }
    u64 fv;
    p1_ffn_pass(P, lds, D, xf, tt, trf + 64 * ((2 * half) ^ q4), accA, accB, csA, csB, P.ws + WS_FQKV + (cg * 16 + tb) * 64, tag, qflag, fv);
    csb[(3 + 2 * half) * 256 + tt * 64 + lane] = csA; csb[(4 + 2 * half) * 256 + tt * 64 + lane] = csB;
    W3 = make_p3w(P, lds0, b, w, lane);
    BAR();
    {
        const bool second = 2 * half + 1 < ntile;
        const float c0 = p1_cs(csb + (3 + 2 * half) * 256, l31), c1 = p1_cs(csb + (4 + 2 * half) * 256, l31);
        u32x4 pk0, pk1;
        if (!__all(lane > 32 || fv == tag)) poll_flags(qflag, lane <= 32, tag);
        kv_first(P, lds, b, w, lane, qf0, qf1, kn2);
        p1_gelu(C, accA, c0, bf2f(b1a), pk0, pk1); p1_store_h(C, fstart + 2 * half, pk0, pk1);
        if (second) { p1_gelu(C, accB, c1, bf2f(b1b), pk0, pk1); p1_store_h(C, fstart + 2 * half + 1, pk0, pk1); }
        else { asm volatile("buffer_store_dword %0, %0, %1, 0 offen sc1\n\tbuffer_store_dword %0, %0, %1, 0 offen sc1" ::"v"(OOB), "s"(rws) : "memory"); }
    }
    return tag;
}

constexpr int att_aflo(int T, bool PF) { return (!PF || T < 8) ? 0 : (T - 8) * 28 / 24; }
constexpr int att_kv(int T) { return ((T & 1) == 0 && (T >> 1) + 3 < 16) ? 2 : 0; }
constexpr int att_w(int T, bool PF) { return !PF ? 0 : T < 12 ? 1 : T == 28 ? 2 : 0; }
constexpr int att_all(int T0, int T1, bool PF) { int n = 0; for (int T = T0; T < T1; ++T) n += att_kv(T) + att_w(T, PF) + (att_aflo(T + 1, PF) - att_aflo(T, PF)); return n; }
#define EXP4(S, i) do { S[i] = __builtin_amdgcn_exp2f(S[i]); S[i + 1] = __builtin_amdgcn_exp2f(S[i + 1]); S[i + 2] = __builtin_amdgcn_exp2f(S[i + 2]); S[i + 3] = __builtin_amdgcn_exp2f(S[i + 3]); } while (0)
template <int KH, bool PF>
DI void att_fast(LAS char* lds, rsrc_t rws, const P3W& W3, unsigned lane, unsigned w, unsigned src, unsigned wdst, unsigned afsrc, u32x4 qf0, u32x4 qf1, f32x16& o, u32x4 (&af)[28], const char* hflag, char* myh, u64 tag, unsigned& tch0, unsigned& tch1) {
    LAS char* kvb = opaque(lds + RING + KH * 8192 + lane * 16);
    const unsigned vo = lane * 16, vo1 = lane * 16 + 1024;
    src = rfl_pad(src); wdst = rfl_pad(wdst); afsrc = rfl_pad(afsrc);
    const f32x16 zero = {};
    f32x16 sc[2];
    u32x4 k0 = LDF(kvb + ring_off(0)), k1 = LDF(kvb + ring_off(0) + 1024), v0 = LDF(kvb + ring_off(0) + 2048), v1 = LDF(kvb + ring_off(0) + 3072), p1 = {0u, 0u, 0u, 0u}, v1p = {0u, 0u, 0u, 0u};
    sc[0] = mfma16(k0, qf0, zero); sc[0] = mfma16(k1, qf1, sc[0]);
    k0 = LDF(kvb + ring_off(0) + 4096); k1 = LDF(kvb + ring_off(0) + 4096 + 1024);
    u64 hv;
    static_for<32>([&](auto TC) {
        constexpr int T = decltype(TC)::value, c = T >> 1;
        f32x16& S = sc[T & 1]; f32x16& SN = sc[(T + 1) & 1];
        constexpr int o1 = ring_off((T + 1) >> 1) + ((T + 1) & 1) * 4096, o2 = ring_off((T + 2) >> 1) + ((T + 2) & 1) * 4096;
        u32x4 p0, k0n, k1n, v0n, v1n;
        auto chunk_barrier = [&](auto OPS) {
            constexpr int younger = decltype(OPS)::value;
            if constexpr (PF && T == 7 && KH == 0) {
                if (w == 0) { PIN(p1); PIN(o); asm volatile("s_waitcnt vmcnt(%1)" : "+v"(hv) : "n"(younger)); if (!__all(lane >= 16 || hv == tag)) poll_flags(hflag, lane < 16, tag); }
            }
            asm volatile("s_waitcnt vmcnt(%0) lgkmcnt(%1)\n\ts_barrier" ::"n"(younger), "n"(KH == 0 ? 4 : 2) : "memory");
            if constexpr (T == 1 && KH == 0) { if (w == 0) { PIN(p1); PIN(o); if (lane == 0) st_flag(myh, tag); } }
        };
        if constexpr (PF && T == 4 && KH == 0) ld_flag_async(hv, hflag);
        S[0] = __builtin_amdgcn_exp2f(S[0]); S[1] = __builtin_amdgcn_exp2f(S[1]);
        if (T > 0) o = mfma16(v1p, p1, o);
        S[2] = __builtin_amdgcn_exp2f(S[2]); S[3] = __builtin_amdgcn_exp2f(S[3]);
        if (T + 2 < 32) { k0n = LDF(kvb + o2); k1n = LDF(kvb + o2 + 1024); }
        p0[0] = cvtpk(S[0], S[1]); p0[1] = cvtpk(S[2], S[3]); SBAR();
        if (T + 1 < 32) SN = mfma16(k0, qf0, zero);
        EXP4(S, 4);
        if constexpr (att_kv(T) != 0) { dma16_sc1(rws, vo, src + (c + 3) * 4096, wdst + ring_off(c + 3)); dma16_sc1(rws, vo1, src + (c + 3) * 4096, wdst + ring_off(c + 3) + 1024); }
        if constexpr (PF && T < 12) p3_dma<T / 6, T % 6>(W3);
        if constexpr (PF && T == 28) { tch0 = __builtin_amdgcn_raw_buffer_load_b32(W3.rw2, W3.vt2, 0, 0); tch1 = __builtin_amdgcn_raw_buffer_load_b32(W3.rwo, W3.vto, 0, 0); }
        static_for<att_aflo(T + 1, PF) - att_aflo(T, PF)>([&](auto JC) { constexpr int j = att_aflo(T, PF) + decltype(JC)::value; af[j] = ld16(rws, vo + (j & 3) * 1024, afsrc + (j >> 2) * 4096); });
        p0[2] = cvtpk(S[4], S[5]); p0[3] = cvtpk(S[6], S[7]); SBAR();
        constexpr int younger = c >= 1 ? att_all(2 * c - 2, T + 1, PF) - att_kv(2 * c - 2) : att_all(0, T + 1, PF);
        if constexpr (KH == 1 && (T & 1) == 1 && T < 31) chunk_barrier(IC<younger>{});
        if (T + 1 < 32) SN = mfma16(k1, qf1, SN);
        EXP4(S, 8);
        if (T + 1 < 32) { v0n = LDF(kvb + o1 + 2048); v1n = LDF(kvb + o1 + 3072); }
        p1[0] = cvtpk(S[8], S[9]); p1[1] = cvtpk(S[10], S[11]); SBAR();
        o = mfma16(v0, p0, o);
        EXP4(S, 12);
        p1[2] = cvtpk(S[12], S[13]); p1[3] = cvtpk(S[14], S[15]); SBAR();
        v1p = v1;
        if (T + 2 < 32) { k0 = k0n; k1 = k1n; }
        if (T + 1 < 32) { v0 = v0n; v1 = v1n; }
        if constexpr (KH == 0 && (T & 1) == 1 && T < 31) chunk_barrier(IC<younger>{});
    });
    o = mfma16(v1p, p1, o);
}

DI void keep2(unsigned a, unsigned b) { asm volatile("" ::"v"(a), "v"(b)); }
DI void phase23(const Ptrs& P, LAS char* lds, int b, u64 tag, u32x4 qf0, u32x4 qf1, float kn2, const P3W& W3) {
    const unsigned tid = threadIdx.x, lane = tid & 63, l31 = lane & 31, h = lane >> 5;
    const unsigned w = rfl(tid >> 6), qt = w & 3, kh = w >> 2;
    const unsigned g = b & 7, r = b >> 3, head = 2 * g + (r >> 4), qb = r & 15, tile = qb * 4 + qt;
    const unsigned lds0 = (unsigned)(size_t)lds;
    const rsrc_t rws = mk_rsrc(P.ws, WS_BYTES);
    const bool p3 = r < 28;
    const unsigned tt3 = w & 1, q3 = w >> 1, tb3 = 4 * g + (r & 3), cg3 = p3 ? r >> 2 : 0, tile3 = tb3 * 2 + tt3;
    const unsigned src = rfl_pad(((w & 1) ? WS_V : WS_K) + (head * 64 + kh * 32 + ((w >> 1) & 1)) * 2048);
    const unsigned wdst = rfl_pad(lds0 + RING + w * 2048);
#define STAGE(c, off) do { dma16_sc1(rws, lane * 16, src + (c) * 4096, wdst + (off)); dma16_sc1(rws, lane * 16 + 1024, src + (c) * 4096, wdst + (off) + 1024); } while (0)
    asm volatile("s_waitcnt vmcnt(4)" : "+v"(qf0), "+v"(qf1), "+v"(kn2)::"memory");
    kn2 = swap_max(max32(kn2));
    float qn2 = 0.f;
#pragma unroll
    for (int e = 0; e < 4; ++e) { qn2 = dot2(qf0[e], qf0[e], qn2); qn2 = dot2(qf1[e], qf1[e], qn2); }
    qn2 = swap_add(qn2);
    const float bound = __builtin_amdgcn_sqrtf(qn2 * kn2) * 1.01f + 0.01f;
    const bool safe = __all(bound <= SAFE_BOUND_V);
    float mrun;
    if (safe) {
        const unsigned nb = cvtpk(-bound, 0.f) & 0xffffu;
        if (h) qf1[2] = (qf1[2] & 0xffff0000u) | nb;
        mrun = __uint_as_float((nb & 0x7fffu) << 16);
    } else mrun = -INFINITY;
    PIN(qf1); PIN(mrun);
    STAGE(2, ring_off(2));
    BAR();
    char* myh = P.ws + WS_FH + ((4 * (g >> 1) + (r & 3)) * 16 + 8 * (g & 1) + (r >> 2)) * 64;
    f32x16 o = {};
    u32x4 af[28];
    unsigned tch0 = 0, tch1 = 0;
    const unsigned afsrc = WS_H + (tile3 * 112 + 28 * q3) * 1024;
    const char* hflag = P.ws + WS_FH + ((tb3 >> 1) * 16 + (lane & 15)) * 64;
    if (safe) {
        if (p3) {
            if (kh == 0) att_fast<0, true>(lds, rws, W3, lane, w, src, wdst, afsrc, qf0, qf1, o, af, hflag, myh, tag, tch0, tch1);
            else att_fast<1, true>(lds, rws, W3, lane, w, src, wdst, afsrc, qf0, qf1, o, af, hflag, myh, tag, tch0, tch1);
        } else {
            if (kh == 0) att_fast<0, false>(lds, rws, W3, lane, w, src, wdst, afsrc, qf0, qf1, o, af, hflag, myh, tag, tch0, tch1);
            else att_fast<1, false>(lds, rws, W3, lane, w, src, wdst, afsrc, qf0, qf1, o, af, hflag, myh, tag, tch0, tch1);
        }
    } else {
        const f32x16 zero = {};
        LAS char* kvb = lds + RING + kh * 8192 + lane * 16;
        static_for<12>([&](auto TC) { constexpr int T = decltype(TC)::value; p3_dma<T / 6, T % 6>(W3); });
#pragma unroll 1
        for (unsigned c = 0; c < 16; ++c) {
            auto roff = [](unsigned c) { return (c & 3) == 2 ? 0u : (c & 3) == 0 ? 16384u : (c & 3) == 1 ? 32768u : 49152u; };
            if (c + 3 < 16) STAGE(c + 3, roff(c + 3));
#pragma unroll 1
            for (int t = 0; t < 2; ++t) {
                LAS char* kv = kvb + roff(c) + t * 4096;
                const u32x4 k0 = LDF(kv), k1 = LDF(kv + 1024), v0 = LDF(kv + 2048), v1 = LDF(kv + 3072);
                f32x16 s = mfma16(k0, qf0, zero); s = mfma16(k1, qf1, s);
                float m = s[0];
#pragma unroll
                for (int i = 1; i < 16; ++i) m = fmaxf(m, s[i]);
                m = fmaxf(swap_max(m), mrun);
                const float f = __builtin_amdgcn_exp2f(mrun - m);
                mrun = m;
#pragma unroll
                for (int i = 0; i < 16; ++i) { s[i] = __builtin_amdgcn_exp2f(s[i] - m); o[i] *= f; }
                o = mfma16(v0, pack8(s, 0), o); o = mfma16(v1, pack8(s, 1), o);
            }
            if (c == 2 && w == 0) poll_flags(hflag, lane < 16, tag);
            if (c < 15) VM0_BAR();
            if (c == 0 && tid == 0) st_flag(myh, tag);
        }
#pragma unroll
        for (int j = 0; j < 28; ++j) af[j] = ld16(rws, lane * 16, afsrc + j * 1024);
    }
    BAR();
    LAS float* ex = (LAS float*)(lds + 147456 + qt * 4096);
    if (kh == 1) {
        if (h) o[15] = mrun;
#pragma unroll
        for (int i = 0; i < 16; ++i) ex[i * 64 + lane] = o[i];
    }
    BAR();
    if (kh == 0) {
        const float m2 = ex[15 * 64 + 32 + l31], m = fmaxf(mrun, m2), f1 = __builtin_amdgcn_exp2f(mrun - m), f2 = __builtin_amdgcn_exp2f(m2 - m);
#pragma unroll
        for (int i = 0; i < 16; ++i) o[i] = o[i] * f1 + ex[i * 64 + lane] * f2;
        auto sw = __builtin_amdgcn_permlane32_swap(__float_as_uint(o[12]), __float_as_uint(o[12]), false, false);
        const float l = __uint_as_float(sw[1]);
        const float il = __builtin_amdgcn_rcpf(l);
#pragma unroll
        for (int i = 0; i < 16; ++i) o[i] *= il;
        if (h) { o[12] = 0.f; o[13] = 0.f; o[14] = 0.f; o[15] = 0.f; }
        const unsigned off = WS_O + (tile * 16 + head) * 2048 + lane * 16;
        st16(rws, off, pack8(o, 0)); st16(rws, off + 1024, pack8(o, 1));
    }
    if (!p3) {
        VM0_BAR();
        if (tid == 0) st_flag(P.ws + WS_FO + (qb * 16 + head) * 64, tag);
        if (b == 255 && w == 0) {
#pragma unroll 1
            for (int i = 0; i < 4; ++i) poll_flags(P.ws + WS_FQKV + (i * 64 + lane) * 64, true, tag);
            if (lane == 0) st_flag(P.ws + WS_EPOCH, tag);
        }
        return;
    }
    const unsigned q4 = (lane & 15) >> 2, trb = (4 * (lane >> 5) + q4) * 128 + ((lane >> 4) & 1) * 32 + (lane & 3) * 8 + q3 * 12 * 1024;
    const unsigned tro0 = trb + ((q4 >> 1) & 1) * 64, tro1 = trb + (((q4 >> 1) & 1) ^ 1) * 64;
    const unsigned tok = tile3 * 32 + l31, n0 = cg3 * 64 + q3 * 16;
    u32x2 xr[2], br[2];
#pragma unroll
    for (int gq = 0; gq < 2; ++gq) {
        const unsigned n = n0 + 8 * gq + 4 * h;
        xr[gq] = *(const u32x2*)((const char*)P.x + (tok * DM + n) * 2); br[gq] = *(const u32x2*)((const char*)P.b2 + n * 2);
    }
    f32x16 acc0 = {}, acc1 = {};
    u32x4 ao[8];
    const char* oflag = P.ws + WS_FO + ((tb3 >> 1) * 16 + (lane & 15)) * 64;
    u64 ov;
    static_for<6>([&](auto SC) {
        constexpr int sb = decltype(SC)::value;
        LAS char* wp0 = opaque(lds + p3_region(sb) + tro0); LAS char* wp1 = opaque(lds + p3_region(sb) + tro1);
        if (sb == 2) ld_flag_async(ov, oflag);
        u32x4 w0[3], w1[3];
#pragma unroll
        for (int i = 0; i < 3; ++i) { w0[i] = tr_frag<1024>(wp0 + i * 2048); w1[i] = tr_frag<1024>(wp1 + i * 2048); }
        if (sb == 3) {
#pragma unroll
            for (int j = 0; j < 8; ++j) ao[j] = ld16(rws, lane * 16 + (j & 3) * 1024, WS_O + (tile3 * 32 + 8 * q3) * 1024 + (j >> 2) * 4096);
        }
        SBAR();
        static_for<6>([&](auto IC_) {
            constexpr int i = decltype(IC_)::value, j = 6 * sb + i;
            const u32x4 a = j < 28 ? af[j < 28 ? j : 0] : ao[j >= 28 ? j - 28 : 0];
            acc0 = mfma16(w0[i % 3], a, acc0);
            if constexpr (sb + 2 < 6) p3_dma<(sb + 2 < 6 ? sb + 2 : 0), i>(W3);
            acc1 = mfma16(w1[i % 3], a, acc1);
            if (i + 3 < 6) { w0[i % 3] = tr_frag<1024>(wp0 + (i + 3) * 2048); w1[i % 3] = tr_frag<1024>(wp1 + (i + 3) * 2048); }
            SBAR();
        });
        if (sb == 2 && w == 0) {
            asm volatile("s_waitcnt vmcnt(6)" : "+v"(ov));
            if (!__all(lane >= 16 || ov == tag)) poll_flags(oflag, lane < 16, tag);
        }
        if (sb < 4) asm volatile("s_waitcnt vmcnt(6) lgkmcnt(0)\n\ts_barrier" ::: "memory");
        else if (sb == 4) VM0_BAR();
        if (sb == 0 && tid == 0) st_flag(P.ws + WS_FO + (qb * 16 + head) * 64, tag);
        if (sb == 1) keep2(tch0, tch1);
    });
    BAR();
    LAS float* rx = (LAS float*)lds;
    float v[8];
    auto scatter = [&](auto QC_) {
        constexpr int Q = decltype(QC_)::value;
#pragma unroll
        for (int d = 0; d < 4; ++d) {
            if (d != Q) {
#pragma unroll
                for (int i = 0; i < 8; ++i) rx[(((tt3 * 4 + d) * 4 + Q) * 8 + i) * 64 + lane] = (d >> 1) ? acc1[8 * (d & 1) + i] : acc0[8 * (d & 1) + i];
            } else {
#pragma unroll
                for (int i = 0; i < 8; ++i) v[i] = (d >> 1) ? acc1[8 * (d & 1) + i] : acc0[8 * (d & 1) + i];
            }
        }
    };
    if (q3 == 0) scatter(IC<0>{}); else if (q3 == 1) scatter(IC<1>{}); else if (q3 == 2) scatter(IC<2>{}); else scatter(IC<3>{});
    BAR();
#pragma unroll
    for (int s = 1; s < 4; ++s) {
        const unsigned sq = (q3 + s) & 3;
#pragma unroll
        for (int i = 0; i < 8; ++i) v[i] += rx[(((tt3 * 4 + q3) * 4 + sq) * 8 + i) * 64 + lane];
    }
    u32x2 pk[2];
#pragma unroll
    for (int gq = 0; gq < 2; ++gq) {
        const float y0 = v[4 * gq] + __uint_as_float(xr[gq][0] << 16) + __uint_as_float(br[gq][0] << 16), y1 = v[4 * gq + 1] + __uint_as_float(xr[gq][0] & 0xffff0000u) + __uint_as_float(br[gq][0] & 0xffff0000u);
        const float y2 = v[4 * gq + 2] + __uint_as_float(xr[gq][1] << 16) + __uint_as_float(br[gq][1] << 16), y3 = v[4 * gq + 3] + __uint_as_float(xr[gq][1] & 0xffff0000u) + __uint_as_float(br[gq][1] & 0xffff0000u);
        pk[gq] = u32x2{cvtpk(y0, y1), cvtpk(y2, y3)};
    }
    {
        auto s0 = __builtin_amdgcn_permlane32_swap(pk[0][0], pk[1][0], false, false), s1 = __builtin_amdgcn_permlane32_swap(pk[0][1], pk[1][1], false, false);
        const u32x4 o4 = {s0[0], s1[0], s0[1], s1[1]};
        __builtin_amdgcn_raw_buffer_store_b128(o4, mk_rsrc(P.out, SEQ * DM * 2), (tok * DM + n0 + 8 * h) * 2, 0, 0);
    }
    asm volatile("fk_code_end:");
}

__global__ __launch_bounds__(512, 2) void fused_kernel(const void* x, const void* wqkv, const void* wo, const void* w1, const void* b1, const void* w2, const void* b2, void* out, char* ws) {
    extern __shared__ __attribute__((aligned(16))) char lds_raw[];
    LAS char* lds = (LAS char*)lds_raw;
    const Ptrs P{x, wqkv, wo, w1, b1, w2, b2, out, ws};
    u32x4 qf0, qf1; float kn2; P3W W3;
    const u64 tag = phase1(P, lds, blockIdx.x, qf0, qf1, kn2, W3);
    phase23(P, lds, blockIdx.x, tag, qf0, qf1, kn2, W3);
}

extern "C" void kernel_launch(void* const* d_in, const int* in_sizes, int n_in, void* d_out, int out_size, void* d_ws, size_t ws_size, hipStream_t stream) {
    (void)in_sizes; (void)n_in; (void)out_size; (void)ws_size;
    (void)hipFuncSetAttribute((const void*)fused_kernel, hipFuncAttributeMaxDynamicSharedMemorySize, LDS_BYTES);
    fused_kernel<<<256, 512, LDS_BYTES, stream>>>(d_in[0], d_in[1], d_in[2], d_in[4], d_in[5], d_in[6], d_in[7], d_out, (char*)d_ws);
}
```

```cpp
#include <hip/hip_runtime.h>
#include <hip/hip_bf16.h>
#include <stdint.h>
#include <type_traits>
#include <utility>

#define DI __device__ __forceinline__
#define LAS __attribute__((address_space(3)))
#define GAS __attribute__((address_space(1)))
typedef float f32x16 __attribute__((ext_vector_type(16)));
typedef float f32x4 __attribute__((ext_vector_type(4)));
typedef float f32x2 __attribute__((ext_vector_type(2)));
typedef __bf16 bf16x8 __attribute__((ext_vector_type(8)));
typedef __bf16 bf16x2 __attribute__((ext_vector_type(2)));
typedef unsigned u32x4 __attribute__((ext_vector_type(4)));
typedef unsigned u32x2 __attribute__((ext_vector_type(2)));
typedef short s16x4 __attribute__((ext_vector_type(4)));
typedef unsigned long long u64;
typedef __amdgpu_buffer_rsrc_t rsrc_t;

constexpr int SEQ = 2048, DM = 448, NH = 16, DH = 28, FF = 1792;
constexpr unsigned WS_EPOCH = 0, WS_FQKV = 4096, WS_FH = WS_FQKV + 256 * 64, WS_FO = WS_FH + 256 * 64;
constexpr unsigned WS_KN2 = 512u << 10;
constexpr unsigned WS_Q = 1u << 20, WS_K = 3u << 20, WS_V = 5u << 20, WS_O = 7u << 20, WS_H = 9u << 20;
constexpr unsigned WS_BYTES = 17u << 20;
constexpr float C2 = 0.27264234f;
constexpr float LN_EPS = 1e-5f;
constexpr int AUX_SC1 = 16;
constexpr unsigned LDS_BYTES = 160 * 1024;
constexpr unsigned SPIN_MAX = 200000;
constexpr float SAFE_BOUND_V = 40.f;
constexpr unsigned OOB = 0x7ffffff0u;

DI rsrc_t mk_rsrc(const void* p, unsigned bytes) { return __builtin_amdgcn_make_buffer_rsrc((void*)p, (short)0, (int)bytes, 0x00020000); }
DI unsigned rfl(unsigned x) { return __builtin_amdgcn_readfirstlane(x); }
DI unsigned rfl_pad(unsigned x) { unsigned r = rfl(x); asm volatile("s_nop 4" : "+s"(r)); return r; }
DI void dma16(rsrc_t rs, unsigned voff, unsigned soff, unsigned ldsaddr) {
    asm volatile("s_mov_b32 m0, %0\n\ts_nop 0\n\tbuffer_load_dwordx4 %1, %2, %3 offen lds" ::"s"(ldsaddr), "v"(voff), "s"(rs), "s"(soff) : "memory");
}
DI void dma16_sc1(rsrc_t rs, unsigned voff, unsigned soff, unsigned ldsaddr) {
    asm volatile("s_mov_b32 m0, %0\n\ts_nop 0\n\tbuffer_load_dwordx4 %1, %2, %3 offen sc1 lds" ::"s"(ldsaddr), "v"(voff), "s"(rs), "s"(soff) : "memory");
}
#define WAIT_VM0() asm volatile("s_waitcnt vmcnt(0)" ::: "memory")
#define BAR() asm volatile("s_waitcnt lgkmcnt(0)\n\ts_barrier" ::: "memory")
#define VM0_BAR() asm volatile("s_waitcnt vmcnt(0) lgkmcnt(0)\n\ts_barrier" ::: "memory")
#define VMCNT(n) asm volatile("s_waitcnt vmcnt(%0)" ::"n"(n) : "memory")
#define VMCNT_BAR(n) asm volatile("s_waitcnt vmcnt(%0) lgkmcnt(0)\n\ts_barrier" ::"n"(n) : "memory")
#define SBAR() __builtin_amdgcn_sched_barrier(0)
#define PIN(x) asm volatile("" : "+v"(x))

template <int V> using IC = std::integral_constant<int, V>;
template <int... Is, class F> DI void static_for_(std::integer_sequence<int, Is...>, F&& f) { (f(IC<Is>{}), ...); }
template <int N, class F> DI void static_for(F&& f) { static_for_(std::make_integer_sequence<int, N>{}, f); }

DI f32x16 mfma16(u32x4 a, u32x4 b, f32x16 c) { return __builtin_amdgcn_mfma_f32_32x32x16_bf16(__builtin_bit_cast(bf16x8, a), __builtin_bit_cast(bf16x8, b), c, 0, 0, 0); }
DI f32x16 mfma_f32(float a, float b, f32x16 c) { return __builtin_amdgcn_mfma_f32_32x32x2f32(a, b, c, 0, 0, 0); }
DI unsigned cvtpk(float lo, float hi) { f32x2 v = {lo, hi}; return __builtin_bit_cast(unsigned, __builtin_convertvector(v, bf16x2)); }
DI float dot2(unsigned a, unsigned b, float c) { return __builtin_amdgcn_fdot2_f32_bf16(__builtin_bit_cast(bf16x2, a), __builtin_bit_cast(bf16x2, b), c, false); }
DI float swap_add(float x) { auto r = __builtin_amdgcn_permlane32_swap(__float_as_uint(x), __float_as_uint(x), false, false); return __uint_as_float(r[0]) + __uint_as_float(r[1]); }
DI float swap_max(float x) { auto r = __builtin_amdgcn_permlane32_swap(__float_as_uint(x), __float_as_uint(x), false, false); return fmaxf(__uint_as_float(r[0]), __uint_as_float(r[1])); }
template <int CTRL> DI float dpp_max(float x) { return fmaxf(x, __uint_as_float(__builtin_amdgcn_update_dpp(0u, __float_as_uint(x), CTRL, 0xf, 0xf, true))); }
DI float max32(float x) {
    x = dpp_max<0x128>(x); x = dpp_max<0x124>(x); x = dpp_max<0x122>(x); x = dpp_max<0x121>(x);
    auto sw = __builtin_amdgcn_permlane16_swap(__float_as_uint(x), __float_as_uint(x), false, false);
    return fmaxf(__uint_as_float(sw[0]), __uint_as_float(sw[1]));
}
template <int D2> DI u32x4 tr_frag(LAS char* p) {
    s16x4 lo = __builtin_amdgcn_ds_read_tr16_b64_v4i16((LAS s16x4*)p), hi = __builtin_amdgcn_ds_read_tr16_b64_v4i16((LAS s16x4*)(p + D2));
    u32x2 a = __builtin_bit_cast(u32x2, lo), b = __builtin_bit_cast(u32x2, hi);
    return u32x4{a[0], a[1], b[0], b[1]};
}
DI u32x4 pack8(const f32x16& v, int s) { return u32x4{cvtpk(v[8 * s], v[8 * s + 1]), cvtpk(v[8 * s + 2], v[8 * s + 3]), cvtpk(v[8 * s + 4], v[8 * s + 5]), cvtpk(v[8 * s + 6], v[8 * s + 7])}; }
DI void st16(rsrc_t rs, unsigned off, u32x4 v) { __builtin_amdgcn_raw_buffer_store_b128(v, rs, off, 0, AUX_SC1); }
DI u32x4 ld16(rsrc_t rs, unsigned voff, unsigned soff) { return __builtin_amdgcn_raw_buffer_load_b128(rs, voff, soff, AUX_SC1); }
DI float bf2f(unsigned short b) { return __uint_as_float((unsigned)b << 16); }
DI u64 ld_flag(const char* p) { return __hip_atomic_load((GAS const u64*)p, __ATOMIC_RELAXED, __HIP_MEMORY_SCOPE_AGENT); }
DI void st_flag(char* p, u64 v) { __hip_atomic_store((GAS u64*)p, v, __ATOMIC_RELAXED, __HIP_MEMORY_SCOPE_AGENT); }
DI void poll_flags(const char* p, bool use, u64 tag) {
    for (unsigned n = 0; n < SPIN_MAX; ++n) {
        const u64 v = use ? ld_flag(p) : tag;
        if (__all(v == tag)) break;
        __builtin_amdgcn_s_sleep(1);
    }
    asm volatile("" ::: "memory");
}
template <class T> DI T* opaque(T* p) { asm volatile("" : "+v"(p)); return p; }
#define LDF(p) (*(LAS u32x4*)(p))

struct Ptrs { const void* x; const void* wqkv; const void* wo; const void* w1; const void* b1; const void* w2; const void* b2; void* out; char* ws; };

constexpr unsigned L_STAT = 155648, L_CS = 156672;
DI void sum8(const u32x4& f, float& a, float& b) {
    float t0, t1, t2, t3;
    asm volatile("v_lshlrev_b32 %2, 16, %6\n\tv_and_b32 %3, 0xffff0000, %6\n\tv_lshlrev_b32 %4, 16, %7\n\tv_and_b32 %5, 0xffff0000, %7\n\t"
                 "v_add_f32 %0, %0, %2\n\tv_add_f32 %1, %1, %3\n\tv_add_f32 %0, %0, %4\n\tv_add_f32 %1, %1, %5\n\t"
                 "v_lshlrev_b32 %2, 16, %8\n\tv_and_b32 %3, 0xffff0000, %8\n\tv_lshlrev_b32 %4, 16, %9\n\tv_and_b32 %5, 0xffff0000, %9\n\t"
                 "v_add_f32 %0, %0, %2\n\tv_add_f32 %1, %1, %3\n\tv_add_f32 %0, %0, %4\n\tv_add_f32 %1, %1, %5"
                 : "+v"(a), "+v"(b), "=&v"(t0), "=&v"(t1), "=&v"(t2), "=&v"(t3) : "v"(f[0]), "v"(f[1]), "v"(f[2]), "v"(f[3]));
}
struct P1Dma { rsrc_t rqkv, rw1, rw2, rwo; unsigned vq0, vq1, vq2, vf, qsoff, qdst, fsoff, fdst, tsoff, tdst, usoff, udst, tdummy, vt2, vto; };

struct QkvAddr { LAS char* qa[4]; LAS char* qa2[4]; LAS char* xu[4]; };
template <int HALF, bool SWA, bool SWB>
DI void p1_qkv_pass(const Ptrs& P, const P1Dma& D, u32x4 (&xf)[28], const QkvAddr& A, bool is28, f32x16& accA, f32x16& accB, float& csA, float& csB) {
    constexpr int PF = 3;
    u32x4 wa[PF], wb[PF];
    float a0 = 0.f, a1 = 0.f, b0 = 0.f, b1 = 0.f;
    const unsigned ones = 0x3F803F80u;
    accA = f32x16{}; accB = f32x16{};
    auto rd = [&](auto SC) {
        constexpr int s = decltype(SC)::value; constexpr int o = s < 12 ? (s & ~3) * 3072 : ((s & ~3) - 12) * 3072;
        wa[s % PF] = tr_frag<768>((s < 12 ? A.qa[s & 3] : A.qa2[s & 3]) + o); if (HALF == 0) wb[s % PF] = tr_frag<768>((s < 12 ? A.qa[s & 3] : A.qa2[s & 3]) + o + 56);
    };
    rd(IC<0>{}); rd(IC<1>{}); rd(IC<2>{});
    SBAR();
    static_for<28>([&](auto SC) {
        constexpr int s = decltype(SC)::value;
        u32x4 ca = wa[s % PF]; const u32x4 cb = wb[s % PF];
        if (HALF == 1) { ca[0] = is28 ? ones : ca[0]; ca[1] = is28 ? ones : ca[1]; ca[2] = is28 ? ones : ca[2]; ca[3] = is28 ? ones : ca[3]; }
        accA = SWA ? mfma16(xf[s], ca, accA) : mfma16(ca, xf[s], accA);
        if constexpr (s < 6) dma16(D.rqkv, s % 3 == 0 ? D.vq0 : s % 3 == 1 ? D.vq1 : D.vq2, D.qsoff + (s / 3) * (16 * 2688), D.qdst + s * 1024);
        else if constexpr (s >= 12 && s < 20) dma16(D.rw1, D.vf, D.fsoff + (s - 12) * (4 * 3584), D.fdst + (s - 12) * 1024);
        else if constexpr (s >= 20 && s < 24) dma16(D.rw1, D.vf, D.tsoff + (s - 20) * (4 * 3584), D.tdst + (s - 20) * 1024);
        if (HALF == 0) accB = SWB ? mfma16(xf[s], cb, accB) : mfma16(cb, xf[s], accB);
        else accB = mfma16(xf[s], xf[s], accB);
        if constexpr (s + PF < 28 && !(s + PF >= 12 && s <= 11)) rd(IC<(s + PF < 28 ? s + PF : 0)>{});
        if constexpr (s >= 4 && s < 8) {
#pragma unroll
            for (int i = 0; i < 3; ++i) { constexpr int dummy = 0; (void)dummy; const int j = 3 * (s - 4) + i; xf[16 + j] = LDF(A.xu[j & 3] + 128 * (j >> 2)); }
        }
        if constexpr ((s & 3) == 0) { sum8(ca, a0, a1); if (HALF == 0) sum8(cb, b0, b1); }
        SBAR();
        if constexpr (s == 24) {
            asm volatile("s_mov_b32 m0, %0\n\ts_nop 0\n\tbuffer_load_dword %1, %2, 0 offen lds\n\tbuffer_load_dword %3, %4, 0 offen lds" ::"s"(D.tdummy), "v"(D.vt2), "s"(D.rw2), "v"(D.vto), "s"(D.rwo) : "memory");
        }
        if constexpr (s == 3) { VMCNT_BAR(6); SBAR(); }
        if constexpr (s == 11) {
            VMCNT_BAR(0);
            rd(IC<12>{}); rd(IC<13>{}); rd(IC<14>{});
            SBAR();
        }
    });
    csA = a0 + a1; csB = b0 + b1;
}
DI void p1_ffn_pass(const Ptrs& P, LAS char* lds, const P1Dma& D, const u32x4 (&xf)[28], unsigned tt, unsigned off, f32x16& accA, f32x16& accB, float& csA, float& csB,
                    char* flag, u64 tag) {
    LAS char* q[4]; LAS char* q2[4]; LAS char* p[4]; LAS char* p2[4];
#pragma unroll
    for (int u = 0; u < 4; ++u) {
        const unsigned ro = off + ((u + tt) & 3) * 4096;
        q[u] = opaque(lds + 49152 + ro); q2[u] = opaque(lds + 114688 + ro); p[u] = opaque(lds + 49152 + (ro ^ 64)); p2[u] = opaque(lds + 114688 + (ro ^ 64));
    }
    constexpr int PF = 3;
    u32x4 wa[PF], wb[PF];
    float a0 = 0.f, a1 = 0.f, b0 = 0.f, b1 = 0.f;
    accA = f32x16{}; accB = f32x16{};
    auto rd = [&](auto SC) {
        constexpr int s = decltype(SC)::value;
        if constexpr (s < 16) { wa[s % PF] = tr_frag<1024>(q[s & 3] + (s & ~3) * 4096); wb[s % PF] = tr_frag<1024>(p[s & 3] + (s & ~3) * 4096); }
        else if constexpr (s < 24) { wa[s % PF] = tr_frag<1024>(q2[s & 3] + ((s & ~3) - 16) * 4096); wb[s % PF] = tr_frag<1024>(p2[s & 3] + ((s & ~3) - 16) * 4096); }
        else { wa[s % PF] = tr_frag<1024>(q[s & 3] - 49152); wb[s % PF] = tr_frag<1024>(p[s & 3] - 49152); }
    };
    rd(IC<0>{}); rd(IC<1>{}); rd(IC<2>{});
    SBAR();
    static_for<28>([&](auto SC) {
        constexpr int s = decltype(SC)::value;
        const u32x4 ca = wa[s % PF], cb = wb[s % PF];
        accA = mfma16(ca, xf[s], accA);
        accB = mfma16(cb, xf[s], accB);
        if constexpr (s + PF < 28) rd(IC<(s + PF < 28 ? s + PF : 0)>{});
        if constexpr ((s & 3) == 0) sum8(ca, a0, a1);
        if constexpr ((s & 3) == 0) sum8(cb, b0, b1);
        SBAR();
        if constexpr (s == 12) {
            VMCNT_BAR(10);
            SBAR();
        }
        if constexpr (s == 18) {
            VMCNT_BAR(0);
            if (threadIdx.x == 0) st_flag(flag, tag);
            SBAR();
        }
    });
    csA = a0 + a1; csB = b0 + b1;
}
DI float p1_cs(LAS float* csb, unsigned l31) {
    float v[8];
#pragma unroll
    for (int i = 0; i < 8; ++i) v[i] = csb[i * 32 + l31];
    return ((v[0] + v[1]) + (v[2] + v[3])) + ((v[4] + v[5]) + (v[6] + v[7]));
}
struct P1Ctx { rsrc_t rws; char* ws; unsigned lane, l31, h, tile; float mu, sig, rstd; };
DI void p1_gelu(const P1Ctx& C, f32x16 acc, float cs, float bias, u32x4& pk0, u32x4& pk1) {
    acc = mfma_f32(C.h ? bias : cs, C.h ? C.sig : -C.mu, acc);
    const float k0 = 2.302208198f * C.rstd, k1 = 0.1029432397f * C.rstd * C.rstd * C.rstd;
#pragma unroll
    for (int i = 0; i < 16; i += 2) {
        const f32x2 a = f32x2{acc[i], acc[i + 1]}, z = a * __builtin_elementwise_fma(a * a, f32x2{k1, k1}, f32x2{k0, k0});
        const f32x2 d = __builtin_elementwise_fma(f32x2{__builtin_amdgcn_exp2f(-z[0]), __builtin_amdgcn_exp2f(-z[1])}, f32x2{C.sig, C.sig}, f32x2{C.sig, C.sig});
        const f32x2 y = a * f32x2{__builtin_amdgcn_rcpf(d[0]), __builtin_amdgcn_rcpf(d[1])};
        acc[i] = y[0]; acc[i + 1] = y[1];
    }
    pk0 = pack8(acc, 0); pk1 = pack8(acc, 1);
}
DI void p1_store_h(const P1Ctx& C, unsigned ft, u32x4 pk0, u32x4 pk1) {
    const unsigned off = WS_H + (C.tile * 112 + 2 * ft) * 1024 + C.lane * 16;
    st16(C.rws, off, pk0); st16(C.rws, off + 1024, pk1);
}
DI void p1_epi_qkv(const P1Ctx& C, f32x16 acc, float cs, unsigned type, unsigned head) {
    const unsigned h = C.h, l31 = C.l31;
    const bool isv = type == 2;
    const float m0 = h ? 0.f : -C.mu, c0 = h ? 0.f : cs, r0 = h ? 0.f : (type == 0 ? C.rstd * C2 : C.rstd), k0 = (h || l31 >= 28) ? 0.f : 1.f;
    acc = mfma_f32(isv ? m0 : c0, isv ? c0 : m0, acc);
    f32x16 R = {}; R = mfma_f32(isv ? r0 : k0, isv ? k0 : r0, R);
    const float pv = (isv && l31 == 28) ? 1.f : 0.f, p12 = isv ? pv : ((type == 1 && h) ? 1.f : 0.f);
    f32x2 n2 = {0.f, 0.f};
#pragma unroll
    for (int i = 0; i < 16; i += 2) {
        const f32x2 t = __builtin_elementwise_fma(f32x2{acc[i], acc[i + 1]}, f32x2{R[i], R[i + 1]}, f32x2{i == 12 ? p12 : pv, pv});
        acc[i] = t[0]; acc[i + 1] = t[1];
        n2 = __builtin_elementwise_fma(t, t, n2);
    }
    const unsigned off = WS_Q + type * (2u << 20) + (head * 64 + C.tile) * 2048 + C.lane * 16;
    st16(C.rws, off, pack8(acc, 0)); st16(C.rws, off + 1024, pack8(acc, 1));
    const float kn = max32(swap_add(n2[0] + n2[1]));
    __builtin_amdgcn_raw_buffer_store_b32(__float_as_uint(kn), C.rws, (type == 1 && C.lane == 0) ? WS_KN2 + (head * 64 + C.tile) * 4 : OOB, 0, AUX_SC1);
}

DI u64 make_tag(u64 e) {
    e = ((u64)rfl((unsigned)(e >> 32)) << 32) | rfl((unsigned)e);
    u64 tag = (e + 0x9E3779B97F4A7C15ull); tag = (tag ^ (tag >> 30)) * 0xBF58476D1CE4E5B9ull; tag = (tag ^ (tag >> 27)) * 0x94D049BB133111EBull; tag ^= tag >> 31;
    return tag;
}
constexpr unsigned p3_region(int sb) { return (sb % 3) * 49152u; }
constexpr unsigned RING = 98304;
constexpr unsigned ring_off(int c) { return c % 4 == 2 ? 0u : c % 4 == 0 ? 16384u : c % 4 == 1 ? 32768u : 49152u; }
struct P3W { rsrc_t rw2, rwo; unsigned vw, vo0, vo1, soff, dst, hd0, vt2, vto; };
template <int SB, int I> DI void p3_dma(const P3W& W) {
    constexpr int j = 6 * SB + I;
    if constexpr (j < 28) dma16(W.rw2, W.vw, W.soff + j * (16 * DM * 2), W.dst + p3_region(SB) + I * 2048);
    else {
        dma16(W.rwo, ((j - 28) & 1) ? W.vo1 : W.vo0, W.hd0 + ((j - 28) >> 1) * (28 * DM * 2), W.dst + p3_region(SB) + I * 2048);
    }
}
DI P3W make_p3w(const Ptrs& P, unsigned lds0, int b, unsigned w, unsigned lane) {
    const unsigned r = b >> 3, cg3 = r < 28 ? r >> 2 : 0;
    P3W W3;
    const unsigned vcol = cg3 * 128 + (((lane >> 2) & 1) ^ ((lane >> 4) & 1)) * 64 + (lane & 3) * 16, rw = (w & 1) * 8 + (lane >> 3);
    W3.rw2 = mk_rsrc(P.w2, FF * DM * 2); W3.rwo = mk_rsrc(P.wo, DM * DM * 2);
    W3.vw = rw * (DM * 2) + vcol; W3.vo0 = W3.vw; W3.vo1 = W3.vw + 16 * (DM * 2);
    W3.soff = rfl_pad(28 * (w >> 1) * (16 * DM * 2)); W3.hd0 = rfl_pad(4 * (w >> 1) * (28 * DM * 2));
    W3.dst = rfl_pad(lds0 + (w >> 1) * 6 * 2048 + (w & 1) * 1024);
    {
        const unsigned L = w * 64 + lane, j = 12 + (L >> 4), row = L & 15, qq = r & 3;
        W3.vt2 = w < 4 ? ((28 * qq + j) * 16 + row) * (DM * 2) + cg3 * 128 : OOB;
        W3.vto = (w == 4 || w == 5) ? (28 * (4 * qq + ((j - 28) >> 1)) + 16 * ((j - 28) & 1) + row) * (DM * 2) + cg3 * 128 : OOB;
    }
    return W3;
}
DI void kv_first(const Ptrs& P, LAS char* lds, int b, unsigned w, unsigned lane, u32x4& qf0, u32x4& qf1, float& kn2) {
    const unsigned g = b & 7, r = b >> 3, head = 2 * g + (r >> 4), tile = (r & 15) * 4 + (w & 3);
    const rsrc_t rws = mk_rsrc(P.ws, WS_BYTES);
    const unsigned src = rfl_pad(((w & 1) ? WS_V : WS_K) + (head * 64 + (w >> 2) * 32 + ((w >> 1) & 1)) * 2048), wdst = rfl_pad((unsigned)(size_t)lds + 98304 + w * 2048);
    dma16_sc1(rws, lane * 16, src, wdst + 16384); dma16_sc1(rws, lane * 16 + 1024, src, wdst + 16384 + 1024);
    dma16_sc1(rws, lane * 16, src + 4096, wdst + 32768); dma16_sc1(rws, lane * 16 + 1024, src + 4096, wdst + 32768 + 1024);
    const unsigned qs = rfl_pad(WS_Q + (head * 64 + tile) * 2048), ks = rfl_pad(WS_KN2 + head * 256);
    asm volatile("buffer_load_dwordx4 %0, %3, %5, %6 offen sc1\n\tbuffer_load_dwordx4 %1, %3, %5, %6 offen offset:1024 sc1\n\tbuffer_load_dword %2, %4, %5, %7 offen sc1"
                 : "=&v"(qf0), "=&v"(qf1), "=&v"(kn2) : "v"(lane * 16), "v"(lane * 4), "s"(rws), "s"(qs), "s"(ks) : "memory");
}
DI u64 phase1(const Ptrs& P, LAS char* lds, int b, u32x4& qf0, u32x4& qf1, float& kn2, P3W& W3) {
    const unsigned tid = threadIdx.x, lane = tid & 63, l31 = lane & 31, h = lane >> 5;
    const unsigned w = rfl(tid >> 6), tt = w & 3, half = w >> 2;
    const unsigned g = b & 7, r = b >> 3, tb = 4 * (g >> 1) + (r & 3), cg = 8 * (g & 1) + (r >> 2);
    const unsigned lds0 = (unsigned)(size_t)lds;
    const rsrc_t rx = mk_rsrc(P.x, SEQ * DM * 2);
    P1Dma D;
    D.rqkv = mk_rsrc(P.wqkv, DM * 3 * DM * 2);
    const unsigned cb = 168 * cg, cb16 = cb & ~15u, dl = cb & 15u;
    {
        const unsigned rowa = 2 * w + h;
        const unsigned va = rowa * 896 + ((l31 ^ (rowa & 15)) << 4), sa = tb * 128 * 896;
#pragma unroll
        for (unsigned i = 0; i < 8; ++i) dma16(rx, va, sa + i * (16 * 896), lds0 + (w + 8 * i) * 1024);
    }
    {
        unsigned long long pc; unsigned len;
        asm volatile("s_getpc_b64 %0\n\tfk_code_here:\n\ts_mov_b32 %1, fk_code_end-fk_code_here" : "=s"(pc), "=s"(len));
        const unsigned per = (len / 128 + 31) / 32, line = r * per + w * 64 + lane;
        const bool on = w * 64 + lane < per && line * 128 < len;
        const unsigned long long a = pc + (on ? line * 128 : 0);
        asm volatile("s_mov_b32 m0, %0\n\ts_nop 0\n\tglobal_load_lds_dword %1, off" ::"s"(lds0 + 151552 + w * 256), "v"(a) : "memory");
    }
    {
        auto vq = [&](unsigned p) { const unsigned row = p / 12, ch = p - row * 12; return ch < 11 ? row * 2688 + cb16 + ch * 16 : OOB; };
        D.vq0 = vq(lane); D.vq1 = vq(64 + lane); D.vq2 = vq(128 + lane);
    }
    const unsigned ntile = ((cg ^ (cg >> 1)) & 1) ? 3 : 4, fstart = 7 * (cg >> 1) + ((cg & 1) ? (((cg >> 1) & 1) ? 3 : 4) : 0);
    D.rw1 = mk_rsrc(P.w1, DM * FF * 2);
    { const unsigned rq = lane >> 4, sl = ((lane >> 2) & 3) ^ rq; D.vf = sl < ntile ? rq * 3584 + (fstart + sl) * 64 + (lane & 3) * 16 : OOB; }
    D.qsoff = (12 + 2 * w) * (16 * 2688); D.qdst = lds0 + 2 * w * 3072;
    D.fsoff = 2 * w * (16 * 3584); D.fdst = lds0 + 49152 + 2 * w * 4096;
    D.tsoff = (256 + 16 * w) * 3584; D.tdst = lds0 + 114688 + w * 4096;
    D.usoff = (384 + 8 * w) * 3584; D.udst = lds0 + 2 * w * 1024;
    D.rw2 = mk_rsrc(P.w2, FF * DM * 2); D.rwo = mk_rsrc(P.wo, DM * DM * 2); D.tdummy = lds0 + 151552 + w * 256;
    { const unsigned i = w * 8 + (lane & 7); D.vt2 = (lane < 8 && i < 49) ? (b * 49 + i) * 128 : OOB; D.vto = (lane < 8 && i < 13) ? (b * 13 + i) * 128 : OOB; }
    const u64 eraw = ld_flag(P.ws + WS_EPOCH);
    const unsigned short b1a = ((const unsigned short*)P.b1)[(fstart + 2 * half) * 32 + l31];
    const unsigned short b1b = ((const unsigned short*)P.b1)[(2 * half + 1 < ntile ? fstart + 2 * half + 1 : fstart) * 32 + l31];
    {
        const unsigned so = w * (16 * 2688), ds = lds0 + 114688 + w * 3072;
        if (w < 4) { dma16(D.rqkv, D.vq0, so, ds); dma16(D.rqkv, D.vq1, so, ds + 1024); dma16(D.rqkv, D.vq2, so, ds + 2048); }
        dma16(D.rqkv, D.vq0, so + 4 * (16 * 2688), ds + 4 * 3072); dma16(D.rqkv, D.vq1, so + 4 * (16 * 2688), ds + 4 * 3072 + 1024); dma16(D.rqkv, D.vq2, so + 4 * (16 * 2688), ds + 4 * 3072 + 2048);
    }
#pragma unroll
    for (unsigned i = 0; i < 6; ++i) {
        const unsigned p = 64 * i + lane, rr = p / 24, c = p - rr * 24, row = 16 * w + rr;
        dma16(rx, row * 896 + 512 + ((c ^ ((row >> 1) & 7)) << 4), tb * 128 * 896, lds0 + 65536 + (6 * w + i) * 1024);
    }
    const rsrc_t rws = mk_rsrc(P.ws, WS_BYTES);
    LAS float* stat = (LAS float*)(lds + L_STAT);
    LAS float* csb = (LAS float*)(lds + L_CS);
    P1Ctx C{rws, P.ws, lane, l31, h, tb * 4 + tt, 0.f, 0.f, 0.f};
    const unsigned G = lane >> 4, q4 = (lane & 15) >> 2;
    const unsigned trq = (8 * (G >> 1) + q4) * 192 + dl + (G & 1) * 32 + (lane & 3) * 8 + half * 112;
    const unsigned trf = (8 * (G >> 1) + q4) * 256 + (G & 1) * 32 + (lane & 3) * 8;
    f32x16 accA, accB; float csA, csB;
    const unsigned tA = 3 * cg + 2 * half, tB = 3 * cg + 1, tyA = tA >> 4, tyB = tB >> 4;
    QkvAddr QA;
    {
        LAS char* xb0 = lds + 65536 + (tt * 32 + l31) * 384; const unsigned gswb = (l31 >> 1) & 7;
#pragma unroll
        for (int u = 0; u < 4; ++u) {
            const unsigned ro = ((u + tt) & 3) * 3072;
            QA.qa[u] = opaque(lds + 114688 + trq + ro); QA.qa2[u] = opaque(lds + trq + ro);
            QA.xu[u] = opaque(xb0 + (((2 * ((u + tt) & 3) + h) ^ gswb) << 4));
        }
    }
    const bool m28 = l31 == 28;
    {
        const unsigned i5 = w * 5 + lane, i7 = w * 7 + lane, g5 = b * 37 + i5;
        const unsigned v5 = (lane < 5 && i5 < 37 && g5 < 9408) ? g5 * 128 : OOB, v7 = (lane < 7 && i7 < 49) ? (b * 49 + i7) * 128 : OOB;
        asm volatile("s_mov_b32 m0, %0\n\ts_nop 0\n\tbuffer_load_dword %1, %2, 0 offen lds\n\tbuffer_load_dword %3, %4, 0 offen lds" ::"s"(D.tdummy), "v"(v5), "s"(D.rqkv), "v"(v7), "s"(D.rw1) : "memory");
    }
    VMCNT_BAR(11);
    u32x4 xf[28];
    {
        LAS char* xa = lds + (tt * 32 + l31) * 512;
#pragma unroll
        for (int u = 0; u < 4; ++u) {
            const unsigned c = 2 * ((u + tt) & 3) + h;
            LAS char* x0 = xa + ((c ^ (l31 & 15)) << 4); LAS char* x1 = xa + (((c + 8) ^ (l31 & 15)) << 4);
            xf[u] = LDF(x0); xf[4 + u] = LDF(x1); xf[8 + u] = LDF(x0 + 256); xf[12 + u] = LDF(x1 + 256);
        }
    }
    const u64 tag = make_tag(eraw);
    VMCNT_BAR(8);
    if (half == 0) {
        if (tyA == 2) p1_qkv_pass<0, true, true>(P, D, xf, QA, m28, accA, accB, csA, csB);
        else if (tyB == 2) p1_qkv_pass<0, false, true>(P, D, xf, QA, m28, accA, accB, csA, csB);
        else p1_qkv_pass<0, false, false>(P, D, xf, QA, m28, accA, accB, csA, csB);
        csb[0 * 256 + tt * 64 + lane] = csA; csb[1 * 256 + tt * 64 + lane] = csB;
    } else {
        float a1;
        if (tyA == 2) {
            p1_qkv_pass<1, true, false>(P, D, xf, QA, m28, accA, accB, csA, csB);
            if (l31 == 28) {
#pragma unroll
                for (int i = 0; i < 16; ++i) stat[(tt * 32 + (i & 3) + 8 * (i >> 2) + 4 * h) * 2] = accA[i];
            }
            asm volatile("s_waitcnt lgkmcnt(0)" ::: "memory");
            a1 = stat[(tt * 32 + l31) * 2];
        } else {
            p1_qkv_pass<1, false, false>(P, D, xf, QA, m28, accA, accB, csA, csB);
            auto sw = __builtin_amdgcn_permlane32_swap(__float_as_uint(accA[12]), __float_as_uint(accA[12]), false, false);
            a1 = __uint_as_float(sw[1]);
        }
        csb[2 * 256 + tt * 64 + lane] = csA;
        const unsigned ri = (l31 & 3) + 4 * (l31 >> 3);
        float dg = 0.f;
#pragma unroll
        for (int i = 0; i < 16; ++i) dg = ri == (unsigned)i ? accB[i] : dg;
        const float a2 = swap_add(h == ((l31 >> 2) & 1) ? dg : 0.f);
        const float m = a1 * (1.f / DM), var = fmaxf(a2 * (1.f / DM) - m * m, 0.f) + LN_EPS;
        if (!h) *(LAS f32x2*)(stat + (tt * 32 + l31) * 2) = f32x2{m, __builtin_amdgcn_rsqf(var)};
    }
    VMCNT_BAR(6);
    dma16(D.rw1, D.vf, D.usoff, D.udst); dma16(D.rw1, D.vf, D.usoff + 4 * 3584, D.udst + 1024);
    { const f32x2 st = *(LAS f32x2*)(stat + (tt * 32 + l31) * 2); C.mu = st[0]; C.rstd = st[1]; C.sig = __builtin_amdgcn_rcpf(st[1]); }
    {
        const float c0 = p1_cs(csb + 2 * half * 256, l31), c1 = p1_cs(csb + 256, l31);
        p1_epi_qkv(C, accA, c0, tyA, tA & 15);
        if (half == 0) p1_epi_qkv(C, accB, c1, tyB, tB & 15);
        else asm volatile("buffer_store_dword %0, %0, %1, 0 offen sc1\n\tbuffer_store_dword %0, %0, %1, 0 offen sc1\n\tbuffer_store_dword %0, %0, %1, 0 offen sc1" ::"v"(OOB), "s"(rws) : "memory");
    }
    const unsigned head2 = 2 * g + (r >> 4), qb2 = r & 15;
    const char* qflag;
    { const unsigned cq = head2 / 3, ck = (16 + head2) / 3, cv = (32 + head2) / 3;
      const unsigned idx = lane < 16 ? ck * 16 + lane : lane < 32 ? cv * 16 + (lane - 16) : cq * 16 + qb2; qflag = P.ws + WS_FQKV + (lane <= 32 ? idx : 0) * 64; }
    p1_ffn_pass(P, lds, D, xf, tt, trf + 64 * ((2 * half) ^ q4), accA, accB, csA, csB, P.ws + WS_FQKV + (cg * 16 + tb) * 64, tag);
    csb[(3 + 2 * half) * 256 + tt * 64 + lane] = csA; csb[(4 + 2 * half) * 256 + tt * 64 + lane] = csB;
    W3 = make_p3w(P, lds0, b, w, lane);
    BAR();
    {
        const bool second = 2 * half + 1 < ntile;
        const float c0 = p1_cs(csb + (3 + 2 * half) * 256, l31), c1 = p1_cs(csb + (4 + 2 * half) * 256, l31);
        u32x4 pk0, pk1;
        poll_flags(qflag, lane <= 32, tag);
        kv_first(P, lds, b, w, lane, qf0, qf1, kn2);
        p1_gelu(C, accA, c0, bf2f(b1a), pk0, pk1); p1_store_h(C, fstart + 2 * half, pk0, pk1);
        if (second) { p1_gelu(C, accB, c1, bf2f(b1b), pk0, pk1); p1_store_h(C, fstart + 2 * half + 1, pk0, pk1); }
        else { asm volatile("buffer_store_dword %0, %0, %1, 0 offen sc1\n\tbuffer_store_dword %0, %0, %1, 0 offen sc1" ::"v"(OOB), "s"(rws) : "memory"); }
    }
    return tag;
}

constexpr int att_aflo(int T, bool PF) { return (!PF || T < 6) ? 0 : (T - 6) * 28 / 26; }
constexpr int att_kv(int T) { return ((T & 1) == 0 && (T >> 1) + 3 < 16) ? 2 : 0; }
constexpr int att_w(int T, bool PF) { return !PF ? 0 : T < 12 ? 1 : T == 28 ? 2 : 0; }
constexpr int att_all(int T0, int T1, bool PF) { int n = 0; for (int T = T0; T < T1; ++T) n += att_kv(T) + att_w(T, PF) + (att_aflo(T + 1, PF) - att_aflo(T, PF)); return n; }
#define EXP4(S, i) do { S[i] = __builtin_amdgcn_exp2f(S[i]); S[i + 1] = __builtin_amdgcn_exp2f(S[i + 1]); S[i + 2] = __builtin_amdgcn_exp2f(S[i + 2]); S[i + 3] = __builtin_amdgcn_exp2f(S[i + 3]); } while (0)
template <int KH, bool PF>
DI void att_fast(LAS char* lds, rsrc_t rws, const P3W& W3, unsigned lane, unsigned w, unsigned src, unsigned wdst, unsigned afsrc, u32x4 qf0, u32x4 qf1, f32x16& o, u32x4 (&af)[28], const char* hflag, char* myh, u64 tag, unsigned& tch0, unsigned& tch1) {
    LAS char* kvb = opaque(lds + RING + KH * 8192 + lane * 16);
    const unsigned vo = lane * 16, vo1 = lane * 16 + 1024;
    src = rfl_pad(src); wdst = rfl_pad(wdst); afsrc = rfl_pad(afsrc);
    const f32x16 zero = {};
    f32x16 sc[2];
    u32x4 k0 = LDF(kvb + ring_off(0)), k1 = LDF(kvb + ring_off(0) + 1024), v0 = LDF(kvb + ring_off(0) + 2048), v1 = LDF(kvb + ring_off(0) + 3072), p1 = {0u, 0u, 0u, 0u}, v1p = {0u, 0u, 0u, 0u};
    sc[0] = mfma16(k0, qf0, zero); sc[0] = mfma16(k1, qf1, sc[0]);
    k0 = LDF(kvb + ring_off(0) + 4096); k1 = LDF(kvb + ring_off(0) + 4096 + 1024);
    static_for<32>([&](auto TC) {
        constexpr int T = decltype(TC)::value, c = T >> 1;
        f32x16& S = sc[T & 1]; f32x16& SN = sc[(T + 1) & 1];
        constexpr int o1 = ring_off((T + 1) >> 1) + ((T + 1) & 1) * 4096, o2 = ring_off((T + 2) >> 1) + ((T + 2) & 1) * 4096;
        u32x4 p0, k0n, k1n, v0n, v1n;
        auto chunk_barrier = [&](auto OPS) {
            constexpr int younger = decltype(OPS)::value;
            if constexpr (PF && T == 5 && KH == 0) { if (w == 0) { PIN(p1); PIN(o); poll_flags(hflag, lane < 16, tag); } }
            asm volatile("s_waitcnt vmcnt(%0) lgkmcnt(%1)\n\ts_barrier" ::"n"(younger), "n"(KH == 0 ? 4 : 2) : "memory");
            if constexpr (T == 1 && KH == 0) { if (w == 0) { PIN(p1); PIN(o); if (lane == 0) st_flag(myh, tag); } }
        };
        S[0] = __builtin_amdgcn_exp2f(S[0]); S[1] = __builtin_amdgcn_exp2f(S[1]);
        if (T > 0) o = mfma16(v1p, p1, o);
        S[2] = __builtin_amdgcn_exp2f(S[2]); S[3] = __builtin_amdgcn_exp2f(S[3]);
        if (T + 2 < 32) { k0n = LDF(kvb + o2); k1n = LDF(kvb + o2 + 1024); }
        p0[0] = cvtpk(S[0], S[1]); p0[1] = cvtpk(S[2], S[3]); SBAR();
        if (T + 1 < 32) SN = mfma16(k0, qf0, zero);
        EXP4(S, 4);
        if constexpr (att_kv(T) != 0) { dma16_sc1(rws, vo, src + (c + 3) * 4096, wdst + ring_off(c + 3)); dma16_sc1(rws, vo1, src + (c + 3) * 4096, wdst + ring_off(c + 3) + 1024); }
        if constexpr (PF && T < 12) p3_dma<T / 6, T % 6>(W3);
        if constexpr (PF && T == 28) { tch0 = __builtin_amdgcn_raw_buffer_load_b32(W3.rw2, W3.vt2, 0, 0); tch1 = __builtin_amdgcn_raw_buffer_load_b32(W3.rwo, W3.vto, 0, 0); }
        static_for<att_aflo(T + 1, PF) - att_aflo(T, PF)>([&](auto JC) { constexpr int j = att_aflo(T, PF) + decltype(JC)::value; af[j] = ld16(rws, vo + (j & 3) * 1024, afsrc + (j >> 2) * 4096); });
        p0[2] = cvtpk(S[4], S[5]); p0[3] = cvtpk(S[6], S[7]); SBAR();
        constexpr int younger = c >= 1 ? att_all(2 * c - 2, T + 1, PF) - att_kv(2 * c - 2) : att_all(0, T + 1, PF);
        if constexpr (KH == 1 && (T & 1) == 1 && T < 31) chunk_barrier(IC<younger>{});
        if (T + 1 < 32) SN = mfma16(k1, qf1, SN);
        EXP4(S, 8);
        if (T + 1 < 32) { v0n = LDF(kvb + o1 + 2048); v1n = LDF(kvb + o1 + 3072); }
        p1[0] = cvtpk(S[8], S[9]); p1[1] = cvtpk(S[10], S[11]); SBAR();
        o = mfma16(v0, p0, o);
        EXP4(S, 12);
        p1[2] = cvtpk(S[12], S[13]); p1[3] = cvtpk(S[14], S[15]); SBAR();
        v1p = v1;
        if (T + 2 < 32) { k0 = k0n; k1 = k1n; }
        if (T + 1 < 32) { v0 = v0n; v1 = v1n; }
        if constexpr (KH == 0 && (T & 1) == 1 && T < 31) chunk_barrier(IC<younger>{});
    });
    o = mfma16(v1p, p1, o);
}

DI void keep2(unsigned a, unsigned b) { asm volatile("" ::"v"(a), "v"(b)); }
DI void phase23(const Ptrs& P, LAS char* lds, int b, u64 tag, u32x4 qf0, u32x4 qf1, float kn2, const P3W& W3) {
    const unsigned tid = threadIdx.x, lane = tid & 63, l31 = lane & 31, h = lane >> 5;
    const unsigned w = rfl(tid >> 6), qt = w & 3, kh = w >> 2;
    const unsigned g = b & 7, r = b >> 3, head = 2 * g + (r >> 4), qb = r & 15, tile = qb * 4 + qt;
    const unsigned lds0 = (unsigned)(size_t)lds;
    const rsrc_t rws = mk_rsrc(P.ws, WS_BYTES);
    const bool p3 = r < 28;
    const unsigned tt3 = w & 1, q3 = w >> 1, tb3 = 4 * g + (r & 3), cg3 = p3 ? r >> 2 : 0, tile3 = tb3 * 2 + tt3;
    const unsigned src = rfl_pad(((w & 1) ? WS_V : WS_K) + (head * 64 + kh * 32 + ((w >> 1) & 1)) * 2048);
    const unsigned wdst = rfl_pad(lds0 + RING + w * 2048);
#define STAGE(c, off) do { dma16_sc1(rws, lane * 16, src + (c) * 4096, wdst + (off)); dma16_sc1(rws, lane * 16 + 1024, src + (c) * 4096, wdst + (off) + 1024); } while (0)
    asm volatile("s_waitcnt vmcnt(4)" : "+v"(qf0), "+v"(qf1), "+v"(kn2)::"memory");
    kn2 = swap_max(max32(kn2));
    float qn2 = 0.f;
#pragma unroll
    for (int e = 0; e < 4; ++e) { qn2 = dot2(qf0[e], qf0[e], qn2); qn2 = dot2(qf1[e], qf1[e], qn2); }
    qn2 = swap_add(qn2);
    const float bound = __builtin_amdgcn_sqrtf(qn2 * kn2) * 1.01f + 0.01f;
    const bool safe = __all(bound <= SAFE_BOUND_V);
    float mrun;
    if (safe) {
        const unsigned nb = cvtpk(-bound, 0.f) & 0xffffu;
        if (h) qf1[2] = (qf1[2] & 0xffff0000u) | nb;
        mrun = __uint_as_float((nb & 0x7fffu) << 16);
    } else mrun = -INFINITY;
    PIN(qf1); PIN(mrun);
    STAGE(2, ring_off(2));
    BAR();
    char* myh = P.ws + WS_FH + ((4 * (g >> 1) + (r & 3)) * 16 + 8 * (g & 1) + (r >> 2)) * 64;
    f32x16 o = {};
    u32x4 af[28];
    unsigned tch0 = 0, tch1 = 0;
    const unsigned afsrc = WS_H + (tile3 * 112 + 28 * q3) * 1024;
    const char* hflag = P.ws + WS_FH + ((tb3 >> 1) * 16 + (lane & 15)) * 64;
    if (safe) {
        if (p3) {
            if (kh == 0) att_fast<0, true>(lds, rws, W3, lane, w, src, wdst, afsrc, qf0, qf1, o, af, hflag, myh, tag, tch0, tch1);
            else att_fast<1, true>(lds, rws, W3, lane, w, src, wdst, afsrc, qf0, qf1, o, af, hflag, myh, tag, tch0, tch1);
        } else {
            if (kh == 0) att_fast<0, false>(lds, rws, W3, lane, w, src, wdst, afsrc, qf0, qf1, o, af, hflag, myh, tag, tch0, tch1);
            else att_fast<1, false>(lds, rws, W3, lane, w, src, wdst, afsrc, qf0, qf1, o, af, hflag, myh, tag, tch0, tch1);
        }
    } else {
        const f32x16 zero = {};
        LAS char* kvb = lds + RING + kh * 8192 + lane * 16;
        static_for<12>([&](auto TC) { constexpr int T = decltype(TC)::value; p3_dma<T / 6, T % 6>(W3); });
#pragma unroll 1
        for (unsigned c = 0; c < 16; ++c) {
            auto roff = [](unsigned c) { return (c & 3) == 2 ? 0u : (c & 3) == 0 ? 16384u : (c & 3) == 1 ? 32768u : 49152u; };
            if (c + 3 < 16) STAGE(c + 3, roff(c + 3));
#pragma unroll 1
            for (int t = 0; t < 2; ++t) {
                LAS char* kv = kvb + roff(c) + t * 4096;
                const u32x4 k0 = LDF(kv), k1 = LDF(kv + 1024), v0 = LDF(kv + 2048), v1 = LDF(kv + 3072);
                f32x16 s = mfma16(k0, qf0, zero); s = mfma16(k1, qf1, s);
                float m = s[0];
#pragma unroll
                for (int i = 1; i < 16; ++i) m = fmaxf(m, s[i]);
                m = fmaxf(swap_max(m), mrun);
                const float f = __builtin_amdgcn_exp2f(mrun - m);
                mrun = m;
#pragma unroll
                for (int i = 0; i < 16; ++i) { s[i] = __builtin_amdgcn_exp2f(s[i] - m); o[i] *= f; }
                o = mfma16(v0, pack8(s, 0), o); o = mfma16(v1, pack8(s, 1), o);
            }
            if (c == 2 && w == 0) poll_flags(hflag, lane < 16, tag);
            if (c < 15) VM0_BAR();
            if (c == 0 && tid == 0) st_flag(myh, tag);
        }
#pragma unroll
        for (int j = 0; j < 28; ++j) af[j] = ld16(rws, lane * 16, afsrc + j * 1024);
    }
    BAR();
    LAS float* ex = (LAS float*)(lds + 147456 + qt * 4096);
    if (kh == 1) {
        if (h) o[15] = mrun;
#pragma unroll
        for (int i = 0; i < 16; ++i) ex[i * 64 + lane] = o[i];
    }
    BAR();
    if (kh == 0) {
        const float m2 = ex[15 * 64 + 32 + l31], m = fmaxf(mrun, m2), f1 = __builtin_amdgcn_exp2f(mrun - m), f2 = __builtin_amdgcn_exp2f(m2 - m);
#pragma unroll
        for (int i = 0; i < 16; ++i) o[i] = o[i] * f1 + ex[i * 64 + lane] * f2;
        auto sw = __builtin_amdgcn_permlane32_swap(__float_as_uint(o[12]), __float_as_uint(o[12]), false, false);
        const float l = __uint_as_float(sw[1]);
        const float il = __builtin_amdgcn_rcpf(l);
#pragma unroll
        for (int i = 0; i < 16; ++i) o[i] *= il;
        if (h) { o[12] = 0.f; o[13] = 0.f; o[14] = 0.f; o[15] = 0.f; }
        const unsigned off = WS_O + (tile * 16 + head) * 2048 + lane * 16;
        st16(rws, off, pack8(o, 0)); st16(rws, off + 1024, pack8(o, 1));
    }
    if (!p3) {
        VM0_BAR();
        if (tid == 0) st_flag(P.ws + WS_FO + (qb * 16 + head) * 64, tag);
        if (b == 255 && w == 0) {
#pragma unroll 1
            for (int i = 0; i < 4; ++i) poll_flags(P.ws + WS_FQKV + (i * 64 + lane) * 64, true, tag);
            if (lane == 0) st_flag(P.ws + WS_EPOCH, tag);
        }
        return;
    }
    const unsigned q4 = (lane & 15) >> 2, trb = (4 * (lane >> 5) + q4) * 128 + ((lane >> 4) & 1) * 32 + (lane & 3) * 8 + q3 * 12 * 1024;
    const unsigned tro0 = trb + ((q4 >> 1) & 1) * 64, tro1 = trb + (((q4 >> 1) & 1) ^ 1) * 64;
    const unsigned tok = tile3 * 32 + l31, n0 = cg3 * 64 + q3 * 16;
    u32x2 xr[2], br[2];
#pragma unroll
    for (int gq = 0; gq < 2; ++gq) {
        const unsigned n = n0 + 8 * gq + 4 * h;
        xr[gq] = *(const u32x2*)((const char*)P.x + (tok * DM + n) * 2); br[gq] = *(const u32x2*)((const char*)P.b2 + n * 2);
    }
    f32x16 acc0 = {}, acc1 = {};
    u32x4 ao[8];
    static_for<6>([&](auto SC) {
        constexpr int sb = decltype(SC)::value;
        LAS char* wp0 = opaque(lds + p3_region(sb) + tro0); LAS char* wp1 = opaque(lds + p3_region(sb) + tro1);
        u32x4 w0[3], w1[3];
#pragma unroll
        for (int i = 0; i < 3; ++i) { w0[i] = tr_frag<1024>(wp0 + i * 2048); w1[i] = tr_frag<1024>(wp1 + i * 2048); }
        if (sb == 3) {
#pragma unroll
            for (int j = 0; j < 8; ++j) ao[j] = ld16(rws, lane * 16 + (j & 3) * 1024, WS_O + (tile3 * 32 + 8 * q3) * 1024 + (j >> 2) * 4096);
        }
        SBAR();
        static_for<6>([&](auto IC_) {
            constexpr int i = decltype(IC_)::value, j = 6 * sb + i;
            const u32x4 a = j < 28 ? af[j < 28 ? j : 0] : ao[j >= 28 ? j - 28 : 0];
            acc0 = mfma16(w0[i % 3], a, acc0);
            if constexpr (sb + 2 < 6) p3_dma<(sb + 2 < 6 ? sb + 2 : 0), i>(W3);
            acc1 = mfma16(w1[i % 3], a, acc1);
            if (i + 3 < 6) { w0[i % 3] = tr_frag<1024>(wp0 + (i + 3) * 2048); w1[i % 3] = tr_frag<1024>(wp1 + (i + 3) * 2048); }
            SBAR();
        });
        if (sb == 2 && w == 0) poll_flags(P.ws + WS_FO + ((tb3 >> 1) * 16 + (lane & 15)) * 64, lane < 16, tag);
        if (sb < 4) asm volatile("s_waitcnt vmcnt(6) lgkmcnt(0)\n\ts_barrier" ::: "memory");
        else if (sb == 4) VM0_BAR();
        if (sb == 0 && tid == 0) st_flag(P.ws + WS_FO + (qb * 16 + head) * 64, tag);
        if (sb == 1) keep2(tch0, tch1);
    });
    BAR();
    LAS float* rx = (LAS float*)lds;
    float v[8];
    auto scatter = [&](auto QC_) {
        constexpr int Q = decltype(QC_)::value;
#pragma unroll
        for (int d = 0; d < 4; ++d) {
            if (d != Q) {
#pragma unroll
                for (int i = 0; i < 8; ++i) rx[(((tt3 * 4 + d) * 4 + Q) * 8 + i) * 64 + lane] = (d >> 1) ? acc1[8 * (d & 1) + i] : acc0[8 * (d & 1) + i];
            } else {
#pragma unroll
                for (int i = 0; i < 8; ++i) v[i] = (d >> 1) ? acc1[8 * (d & 1) + i] : acc0[8 * (d & 1) + i];
            }
        }
    };
    if (q3 == 0) scatter(IC<0>{}); else if (q3 == 1) scatter(IC<1>{}); else if (q3 == 2) scatter(IC<2>{}); else scatter(IC<3>{});
    BAR();
#pragma unroll
    for (int s = 1; s < 4; ++s) {
        const unsigned sq = (q3 + s) & 3;
#pragma unroll
        for (int i = 0; i < 8; ++i) v[i] += rx[(((tt3 * 4 + q3) * 4 + sq) * 8 + i) * 64 + lane];
    }
    u32x2 pk[2];
#pragma unroll
    for (int gq = 0; gq < 2; ++gq) {
        const float y0 = v[4 * gq] + __uint_as_float(xr[gq][0] << 16) + __uint_as_float(br[gq][0] << 16), y1 = v[4 * gq + 1] + __uint_as_float(xr[gq][0] & 0xffff0000u) + __uint_as_float(br[gq][0] & 0xffff0000u);
        const float y2 = v[4 * gq + 2] + __uint_as_float(xr[gq][1] << 16) + __uint_as_float(br[gq][1] << 16), y3 = v[4 * gq + 3] + __uint_as_float(xr[gq][1] & 0xffff0000u) + __uint_as_float(br[gq][1] & 0xffff0000u);
        pk[gq] = u32x2{cvtpk(y0, y1), cvtpk(y2, y3)};
    }
    {
        auto s0 = __builtin_amdgcn_permlane32_swap(pk[0][0], pk[1][0], false, false), s1 = __builtin_amdgcn_permlane32_swap(pk[0][1], pk[1][1], false, false);
        const u32x4 o4 = {s0[0], s1[0], s0[1], s1[1]};
        __builtin_amdgcn_raw_buffer_store_b128(o4, mk_rsrc(P.out, SEQ * DM * 2), (tok * DM + n0 + 8 * h) * 2, 0, 0);
    }
    asm volatile("fk_code_end:");
}

__global__ __launch_bounds__(512, 2) void fused_kernel(const void* x, const void* wqkv, const void* wo, const void* w1, const void* b1, const void* w2, const void* b2, void* out, char* ws) {
    extern __shared__ __attribute__((aligned(16))) char lds_raw[];
    LAS char* lds = (LAS char*)lds_raw;
    const Ptrs P{x, wqkv, wo, w1, b1, w2, b2, out, ws};
    u32x4 qf0, qf1; float kn2; P3W W3;
    const u64 tag = phase1(P, lds, blockIdx.x, qf0, qf1, kn2, W3);
    phase23(P, lds, blockIdx.x, tag, qf0, qf1, kn2, W3);
}

extern "C" void kernel_launch(void* const* d_in, const int* in_sizes, int n_in, void* d_out, int out_size, void* d_ws, size_t ws_size, hipStream_t stream) {
    (void)in_sizes; (void)n_in; (void)out_size; (void)ws_size;
    (void)hipFuncSetAttribute((const void*)fused_kernel, hipFuncAttributeMaxDynamicSharedMemorySize, LDS_BYTES);
    fused_kernel<<<256, 512, LDS_BYTES, stream>>>(d_in[0], d_in[1], d_in[2], d_in[4], d_in[5], d_in[6], d_in[7], d_out, (char*)d_ws);
}
```
